```python
import torch

def _softmax(x, axis=-1):
    return torch.softmax(x, dim=axis)
def _gelu(x):
    return torch.nn.functional.gelu(x, approximate="tanh")
def _silu(x):
    return torch.nn.functional.silu(x)
def _sigmoid(x):
    return torch.sigmoid(x)
def _layernorm(x, axis=-1, eps=1e-5):
    m = x.mean(dim=axis, keepdim=True)
    v = x.var(dim=axis, keepdim=True, unbiased=False)
    return (x - m) / torch.sqrt(v + eps)
def _scatter_add(tgt, idx, src, axis=0):
    return tgt.clone().index_add_(axis, idx, src)
def _segment_sum(src, seg, n):
    out = torch.zeros((n,) + src.shape[1:], dtype=src.dtype)
    return out.index_add_(0, seg, src)
def _pad(x, pw):
    flat = tuple(v for pair in reversed(pw) for v in pair)
    return torch.nn.functional.pad(x, flat)

kernel_name = 'standalone__edge_featured_mpnn_gatedgcn__graph'

def setup_inputs():

    _w = (lambda a: (lambda t: t.to(torch.bfloat16) if t.is_floating_point() else t)(torch.as_tensor(a)))
    N, deg, D, De = 25000, 16, 128, max(0, 1)
    E = N * deg
    return {
        "x": _w((lambda *s: torch.randn(*s, dtype=torch.bfloat16) * 0.4)(N, D)),
        "e_attr": _w((lambda *s: torch.randn(*s, dtype=torch.bfloat16) * 0.8)(E, De)),
        "src": _w((lambda lo, hi, *s: torch.randint(lo, hi, s))(0, N, E).to(torch.int64)),
        "dst": _w((lambda lo, hi, *s: torch.randint(lo, hi, s))(0, N, E).to(torch.int64)),
        "w_a": _w((lambda *s: torch.randn(*s, dtype=torch.bfloat16) * 0.4)(D, D)),
        "w_b": _w((lambda *s: torch.randn(*s, dtype=torch.bfloat16) * 0.4)(D, D)),
        "w_c": _w((lambda *s: torch.randn(*s, dtype=torch.bfloat16) * 0.8)(De, D)),
        "w_u": _w((lambda *s: torch.randn(*s, dtype=torch.bfloat16) * 0.05)(D, D)),
        "w_v": _w((lambda *s: torch.randn(*s, dtype=torch.bfloat16) * 0.05)(D, D)),
    }

def reference(x, e_attr, src, dst, w_a, w_b, w_c, w_u, w_v):
    N, D = x.shape
    hv = x @ w_v
    g = x[dst] @ w_a + x[src] @ w_b + e_attr @ w_c
    eta = _sigmoid(g)
    denom = _segment_sum(eta, dst, N) + 1e-6
    eta_n = eta / denom[dst]
    agg = _segment_sum(eta_n * hv[src], dst, N)
    z = x @ w_u + agg
    relu_z = torch.where(z > 0, z, 0 * z)
    return x + relu_z
```

```cpp
#include <hip/hip_runtime.h>
#include <hip/hip_bf16.h>
#include <stdint.h>

namespace {

typedef float f2 __attribute__((ext_vector_type(2)));
typedef float f4 __attribute__((ext_vector_type(4)));
typedef float f16v __attribute__((ext_vector_type(16)));
typedef unsigned u2 __attribute__((ext_vector_type(2)));
typedef unsigned u4 __attribute__((ext_vector_type(4)));
typedef int i2 __attribute__((ext_vector_type(2)));
typedef __bf16 b8 __attribute__((ext_vector_type(8)));
typedef __bf16 b2 __attribute__((ext_vector_type(2)));
typedef _Float16 h2 __attribute__((ext_vector_type(2)));
typedef unsigned short us;

constexpr int kN = 25000, kE = 400000;
constexpr int kBN = 391;
constexpr int kCE = 1564;
constexpr int kCS = 1568;
constexpr int kRows = 25024;
constexpr int kCap = 40;
constexpr int kRowB = 288;
constexpr int kTiles = 13;
constexpr float kNL2E = -1.4426950408889634f;

constexpr size_t kTabBytes = (size_t)4 * kRows * 128;
constexpr size_t kOffRec32 = kTabBytes;
constexpr size_t kOffRec8 = kOffRec32 + (size_t)256 * kCS * 4;
constexpr size_t kOffSeg = kOffRec8 + (size_t)256 * kCS;

__device__ __forceinline__ unsigned f2u(float f) { return __builtin_bit_cast(unsigned, f); }
__device__ __forceinline__ float u2f(unsigned u) { return __builtin_bit_cast(float, u); }
__device__ __forceinline__ float h_lo(unsigned w) { return (float)__builtin_bit_cast(h2, w).x; }
__device__ __forceinline__ float h_hi(unsigned w) { return (float)__builtin_bit_cast(h2, w).y; }
__device__ __forceinline__ float bf_lo(unsigned w) { return __builtin_bit_cast(float, w << 16); }
__device__ __forceinline__ float bf_hi(unsigned w) { return __builtin_bit_cast(float, w & 0xffff0000u); }
__device__ __forceinline__ unsigned pack_bf(float lo, float hi) {
  b2 v = {(__bf16)lo, (__bf16)hi};
  return __builtin_bit_cast(unsigned, v);
}

__device__ __forceinline__ unsigned wave_scan(unsigned v) {
  v += __builtin_amdgcn_update_dpp(0u, v, 0x111, 0xf, 0xf, true);
  v += __builtin_amdgcn_update_dpp(0u, v, 0x112, 0xf, 0xf, true);
  v += __builtin_amdgcn_update_dpp(0u, v, 0x114, 0xf, 0xf, true);
  v += __builtin_amdgcn_update_dpp(0u, v, 0x118, 0xf, 0xf, true);
  v += __builtin_amdgcn_update_dpp(0u, v, 0x142, 0xa, 0xf, false);
  v += __builtin_amdgcn_update_dpp(0u, v, 0x143, 0xc, 0xf, false);
  return v;
}

__device__ __forceinline__ u4 load_wfrag(const us* __restrict__ w0, const us* __restrict__ w1, int fg, int wave, int lane) {
  const us* w = (wave & 8) ? w1 : w0;
  const us* p = w + (16 * (wave & 7) + 8 * (lane >> 5)) * 128 + fg * 32 + (lane & 31);
  unsigned v[8];
#pragma unroll
  for (int j = 0; j < 8; ++j) v[j] = p[j * 128];
  return u4{v[0] | (v[1] << 16), v[2] | (v[3] << 16), v[4] | (v[5] << 16), v[6] | (v[7] << 16)};
}

__device__ __forceinline__ void load_xtile(const us* __restrict__ x, int row0, int lane, u4 (&xr)[8]) {
#pragma unroll
  for (int j = 0; j < 8; ++j) {
    int r = row0 + 4 * j + (lane >> 4);
    r = r < kN ? r : kN - 1;
    xr[j] = *(const u4*)(x + (size_t)r * 128 + (lane & 15) * 8);
  }
}

__device__ __forceinline__ void tile_gemm(char* stage, const char* wfr, int lane, const u4 (&xr)[8], f16v& a0, f16v& a1) {
#pragma unroll
  for (int j = 0; j < 8; ++j) {
    int r = 4 * j + (lane >> 4);
    *(u4*)(stage + r * 256 + (((lane & 15) ^ (r & 15)) << 4)) = xr[j];
  }
  __builtin_amdgcn_fence(__ATOMIC_RELEASE, "wavefront");
  __builtin_amdgcn_wave_barrier();
  __builtin_amdgcn_fence(__ATOMIC_ACQUIRE, "wavefront");
  int r = lane & 31, hh = lane >> 5;
#pragma unroll
  for (int i = 0; i < 16; ++i) { a0[i] = 0.f; a1[i] = 0.f; }
#pragma unroll
  for (int s = 0; s < 8; ++s) {
    u4 xf = *(const u4*)(stage + r * 256 + (((2 * s + hh) ^ (r & 15)) << 4));
    u4 w0 = *(const u4*)(wfr + s * 1024 + lane * 16);
    u4 w1 = *(const u4*)(wfr + (8 + s) * 1024 + lane * 16);
    a0 = __builtin_amdgcn_mfma_f32_32x32x16_bf16(__builtin_bit_cast(b8, w0), __builtin_bit_cast(b8, xf), a0, 0, 0, 0);
    a1 = __builtin_amdgcn_mfma_f32_32x32x16_bf16(__builtin_bit_cast(b8, w1), __builtin_bit_cast(b8, xf), a1, 0, 0, 0);
  }
  __builtin_amdgcn_fence(__ATOMIC_RELEASE, "wavefront");
  __builtin_amdgcn_wave_barrier();
  __builtin_amdgcn_fence(__ATOMIC_ACQUIRE, "wavefront");
}

constexpr int P_STAGE = 0;
constexpr int P_WFR = kTiles * 8192;
constexpr int P_R32 = P_WFR + 16384;
constexpr int P_R8 = P_R32 + kCS * 4;
constexpr int P_CNT = P_R8 + kCS;
constexpr int P_END = P_CNT + 256;

__global__ void __launch_bounds__(1024) k_prep(const us* __restrict__ x, const us* __restrict__ ea,
                                               const int* __restrict__ src, const int* __restrict__ dst,
                                               const us* __restrict__ wb, const us* __restrict__ wv,
                                               char* __restrict__ ws) {
  __shared__ __attribute__((aligned(16))) char sm[P_END];
  const int t = threadIdx.x, lane = t & 63, wave = __builtin_amdgcn_readfirstlane(t >> 6);
  const int b = blockIdx.x, fg = b & 3, bucket = ((b >> 2) & 1) * 32 + (b >> 3), n0 = bucket * kBN;
  unsigned* cnt = (unsigned*)(sm + P_CNT);

  const int e0 = b * kCE + 2 * t;
  const bool ev = (2 * t < kCE) && (e0 < kE);
  i2 dd = {0, 0}, ss = {0, 0};
  unsigned ee = 0;
  if (ev) {
    dd = *(const i2*)(dst + e0);
    ss = *(const i2*)(src + e0);
    ee = *(const unsigned*)(ea + e0);
  }
  u4 wf = load_wfrag(wb, wv, fg, wave, lane);
  u4 xr[8];
  if (wave < kTiles) load_xtile(x, n0 + wave * 32, lane, xr);

  if (t < 64) cnt[t] = 0;
  __syncthreads();
  unsigned b0 = (unsigned)dd.x / kBN, b1 = (unsigned)dd.y / kBN, r0 = 0, r1 = 0;
  if (ev) {
    r0 = atomicAdd(&cnt[b0], 1u);
    r1 = atomicAdd(&cnt[b1], 1u);
  }
  *(u4*)(sm + P_WFR + t * 16) = wf;
  __syncthreads();
  {
    unsigned c = cnt[lane];
    unsigned ex = wave_scan(c) - c;
    if (wave == 15) ((unsigned*)(ws + kOffSeg))[lane * 256 + (b & 7) * 32 + (b >> 3)] = ex | (c << 16);
    unsigned p0 = __builtin_amdgcn_ds_bpermute(b0 << 2, ex) + r0;
    unsigned p1 = __builtin_amdgcn_ds_bpermute(b1 << 2, ex) + r1;
    if (ev) {
      unsigned l0 = dd.x - b0 * kBN, l1 = dd.y - b1 * kBN;
      ((unsigned*)(sm + P_R32))[p0] = (ee << 16) | ((l0 & 1) << 15) | (unsigned)ss.x;
      ((unsigned*)(sm + P_R32))[p1] = (ee & 0xffff0000u) | ((l1 & 1) << 15) | (unsigned)ss.y;
      ((unsigned char*)(sm + P_R8))[p0] = (unsigned char)(l0 >> 1);
      ((unsigned char*)(sm + P_R8))[p1] = (unsigned char)(l1 >> 1);
    }
  }
  __syncthreads();
  if (wave >= kTiles) {
    const int cslot = (b & 7) * 32 + (b >> 3);
    for (int i = t - kTiles * 64; i < kCS / 4 + kCS / 16; i += 192) {
      u4 v = *(const u4*)(sm + P_R32 + i * 16);
      char* g = i < kCS / 4 ? ws + kOffRec32 + (size_t)cslot * kCS * 4 + i * 16
                            : ws + kOffRec8 + (size_t)cslot * kCS + (i - kCS / 4) * 16;
      *(u4*)g = v;
    }
    if (bucket == 63 && wave == 15 && lane < 8)
      *(u4*)(ws + ((size_t)fg * kRows + kN) * 128 + lane * 16) = u4{0x7c00u, 0x7c00u, 0x7c00u, 0x7c00u};
    return;
  }
  char* stage = sm + P_STAGE + wave * 8192;
  f16v ab, av;
  tile_gemm(stage, sm + P_WFR, lane, xr, ab, av);
  {
    const int r = lane & 31, hh = lane >> 5;
#pragma unroll
    for (int i = 0; i < 4; ++i) {
      u4 o;
#pragma unroll
      for (int j = 0; j < 4; ++j) {
        unsigned lo = (unsigned)__builtin_bit_cast(us, (_Float16)(ab[4 * i + j] * kNL2E));
        unsigned u = f2u(av[4 * i + j]) | 0x8000u;
        unsigned hv = f2u(av[4 * i + j]);
        unsigned m = (hv & 0x7fffffffu) < 0x8000u ? u : hv;
        o[j] = ((m + 0x8000u - lo) & 0xffff0000u) | lo;
      }
      *(u4*)(stage + r * 128 + (((2 * i + hh) ^ (r & 7)) << 4)) = o;
    }
  }
  __builtin_amdgcn_fence(__ATOMIC_RELEASE, "wavefront");
  __builtin_amdgcn_wave_barrier();
  __builtin_amdgcn_fence(__ATOMIC_ACQUIRE, "wavefront");
  char* tab = ws + ((size_t)fg * kRows + n0 + wave * 32) * 128;
#pragma unroll
  for (int j = 0; j < 4; ++j) {
    int r = 8 * j + (lane >> 3);
    u4 v = *(const u4*)(stage + r * 128 + (((lane & 7) ^ (r & 7)) << 4));
    int nl = wave * 32 + r;
    if (nl < kBN && n0 + nl < kN) *(u4*)(tab + r * 128 + (lane & 7) * 16) = v;
  }
}

constexpr int Q_ROWS = 0;
constexpr int Q_WFR = kTiles * 32 * kRowB;
constexpr int Q_CNT = Q_WFR + 16384;
constexpr int Q_ORD = Q_CNT + 416 * 4;
constexpr int Q_HIST = Q_ORD + 416 * 4;
constexpr int Q_MISC = Q_HIST + 256;
constexpr int Q_END = Q_MISC + 16;
constexpr int kSpareRow = 415;

__device__ __forceinline__ f2 pk_fma(f2 a, f2 b, f2 c) { return __builtin_elementwise_fma(a, b, c); }

__device__ __forceinline__ u4 gather(const char* __restrict__ tab, unsigned rec, unsigned qoff) {
  unsigned o;
  asm("v_mad_u32_u16 %0, %1, %2, %3" : "=v"(o) : "v"(rec), "s"(128u), "v"(qoff));
  return *(const u4*)(tab + o);
}

__device__ __forceinline__ void edge_step(u4 w, unsigned rec, const f4& c, f2 ea01, f2 ea23, f2& n01, f2& n23, f2& d01, f2& d23) {
  const float e = h_hi(rec);
  float y[4];
#pragma unroll
  for (int j = 0; j < 4; ++j)
    y[j] = __builtin_amdgcn_exp2f(__builtin_fmaf(e, c[j], h_lo(w[j])));
  f2 p01 = pk_fma(f2{y[0], y[1]}, ea01, f2{1.f, 1.f}), p23 = pk_fma(f2{y[2], y[3]}, ea23, f2{1.f, 1.f});
  f2 g01 = {__builtin_amdgcn_rcpf(p01.x), __builtin_amdgcn_rcpf(p01.y)};
  f2 g23 = {__builtin_amdgcn_rcpf(p23.x), __builtin_amdgcn_rcpf(p23.y)};
  n01 = pk_fma(g01, __builtin_bit_cast(f2, u2{w.x, w.y}), n01);
  n23 = pk_fma(g23, __builtin_bit_cast(f2, u2{w.z, w.w}), n23);
  d01 += g01;
  d23 += g23;
}

__device__ __forceinline__ float finish(float num, float den, float u, float xv) {
  float z = u + num * __builtin_amdgcn_rcpf(den + 1e-6f);
  return xv + (z > 0.f ? z : 0.f * z);
}

__global__ void __launch_bounds__(1024) k_edge(const us* __restrict__ x, const us* __restrict__ wa,
                                               const us* __restrict__ wu, const us* __restrict__ wc,
                                               const us* __restrict__ wb, const char* __restrict__ ws,
                                               us* __restrict__ out) {
  __shared__ __attribute__((aligned(16))) char sm[Q_END];
  const int t = threadIdx.x, lane = t & 63, wave = __builtin_amdgcn_readfirstlane(t >> 6);
  const int b = blockIdx.x, fg = b & 3, bucket = ((b >> 2) & 1) * 32 + (b >> 3), n0 = bucket * kBN;
  const int nb = min(kBN, kN - n0);
  unsigned* cnt = (unsigned*)(sm + Q_CNT);
  unsigned* ord = (unsigned*)(sm + Q_ORD);
  unsigned* hist = (unsigned*)(sm + Q_HIST);
  volatile unsigned* misc = (volatile unsigned*)(sm + Q_MISC);
  const unsigned* __restrict__ seg = (const unsigned*)(ws + kOffSeg) + bucket * 256;
  const unsigned* __restrict__ rec32 = (const unsigned*)(ws + kOffRec32);
  const unsigned char* __restrict__ rec8 = (const unsigned char*)(ws + kOffRec8);
  const char* __restrict__ tab = ws + (size_t)fg * kRows * 128;
  const int q = lane & 7, hh = lane >> 5, l31 = lane & 31;

  unsigned sg[8];
#pragma unroll
  for (int i = 0; i < 8; ++i) {
    unsigned s0 = seg[wave * 16 + 2 * i], s1 = seg[wave * 16 + 2 * i + 1];
    sg[i] = hh ? s1 : s0;
  }
  u4 wf = load_wfrag(wa, wu, fg, wave, lane);
  const u2 craw = *(const u2*)(wc + fg * 32 + 4 * q);
  u4 xr[8];
  if (wave < kTiles) load_xtile(x, n0 + wave * 32, lane, xr);
  unsigned r32[8], r8[8];
#pragma unroll
  for (int i = 0; i < 8; ++i) {
    r32[i] = 0; r8[i] = 0;
    if ((unsigned)l31 < (sg[i] >> 16)) {
      unsigned idx = (wave * 16 + 2 * i + hh) * kCS + (sg[i] & 0xffffu) + l31;
      r32[i] = rec32[idx];
      r8[i] = rec8[idx];
    }
  }
  if (t < 416) cnt[t] = 0;
  if (t >= 512 && t < 576) hist[t - 512] = 0;
  if (t == 640) { misc[0] = 16; misc[1] = 0; }
  *(u4*)(sm + Q_WFR + t * 16) = wf;
  f4 c = {bf_lo(craw.x) * kNL2E, bf_hi(craw.x) * kNL2E, bf_lo(craw.y) * kNL2E, bf_hi(craw.y) * kNL2E};
  float cm = fmaxf(fmaxf(fabsf(c[0]), fabsf(c[1])), fmaxf(fabsf(c[2]), fabsf(c[3])));
  cm = fmaxf(cm, __shfl_xor(cm, 1));
  cm = fmaxf(cm, __shfl_xor(cm, 2));
  cm = fmaxf(cm, __shfl_xor(cm, 4));
  unsigned ce = __builtin_bit_cast(unsigned, cm) & 0x7f800000u;
  bool slow = !(cm < 1e30f);
  ce = (ce < (20u << 23) || slow) ? (127u << 23) : ce;
  const float escale = __builtin_bit_cast(float, ce);
  const f4 cn = c * __builtin_bit_cast(float, (254u << 23) - ce);
  __syncthreads();

  if (wave < kTiles) {
    char* stage = sm + Q_ROWS + wave * 32 * kRowB;
    f16v aa, au;
    tile_gemm(stage, sm + Q_WFR, lane, xr, aa, au);
    float am = 0.f;
#pragma unroll
    for (int i = 0; i < 4; ++i) {
      u4 o;
#pragma unroll
      for (int j = 0; j < 4; ++j) {
        float a = aa[4 * i + j] * kNL2E;
        slow |= !(fabsf(a) <= 80.f);
        o[j] = pack_bf(au[4 * i + j], a);
      }
      *(u4*)(stage + l31 * kRowB + ((2 * i + hh) << 4)) = o;
    }
  }
  __syncthreads();

  auto put = [&](unsigned w32, unsigned w8) {
    unsigned dl = (w8 << 1) | ((w32 >> 15) & 1u);
    float e = bf_hi(w32) * escale;
    slow |= !(fabsf(e) <= 16384.f);
    unsigned rec = ((unsigned)__builtin_bit_cast(us, (_Float16)e) << 16) | (w32 & 0x7fffu);
    unsigned rk = atomicAdd(&cnt[dl], 1u);
    if (rk < (unsigned)kCap) *(unsigned*)(sm + Q_ROWS + dl * kRowB + 128 + rk * 4) = rec;
    else slow = true;
  };
  unsigned mx = 0;
#pragma unroll
  for (int i = 0; i < 8; ++i) {
    if ((unsigned)l31 < (sg[i] >> 16)) put(r32[i], r8[i]);
    mx = max(mx, sg[i] >> 16);
  }
  if (__any(mx > 32u)) {
#pragma unroll 1
    for (int i = 0; i < 16; ++i) {
      unsigned s = seg[wave * 16 + i];
      for (unsigned j = 32 + lane; j < (s >> 16); j += 64) {
        unsigned idx = (wave * 16 + i) * kCS + (s & 0xffffu) + j;
        put(rec32[idx], rec8[idx]);
      }
    }
  }
  if (slow) misc[1] = 1;
  __syncthreads();

  if (misc[1] == 0) {
    unsigned deg = 0, rk = 0;
    if (t < nb) {
      deg = cnt[t];
      rk = atomicAdd(&hist[deg], 1u);
    }
    __syncthreads();
    if (wave < 7) {
      unsigned above = nb - wave_scan(hist[lane]);
      unsigned pos = __builtin_amdgcn_ds_bpermute(deg << 2, above) + rk;
      if (t < nb) ord[pos] = t | (deg << 16);
      else if (t < 416) ord[t] = kSpareRow;
    }
    __syncthreads();

    const int ng = (nb + 7) >> 3;
    const unsigned qoff = q * 16;
    int g = wave;
    while (g < ng) {
      const unsigned ent = ord[g * 8 + (lane >> 3)];
      unsigned gn = 0;
      if (lane == 0) gn = atomicAdd((unsigned*)&misc[0], 1u);
      const unsigned node = ent & 0xffffu, dg = ent >> 16;
      const int w = __builtin_amdgcn_readfirstlane(dg);
      const int nch = (w + 3) >> 2;
      char* row = sm + Q_ROWS + node * kRowB;
      for (unsigned j = dg + q; j < 4u * nch; j += 8) *(unsigned*)(row + 128 + j * 4) = (unsigned)kN;
      __builtin_amdgcn_fence(__ATOMIC_RELEASE, "wavefront");
      __builtin_amdgcn_wave_barrier();
      __builtin_amdgcn_fence(__ATOMIC_ACQUIRE, "wavefront");
      const u4 au = *(const u4*)(row + qoff);
      const unsigned gnode = min(n0 + (int)node, kN - 1);
      const size_t xo = (size_t)gnode * 128 + fg * 32 + 4 * q;
      const u2 xs = *(const u2*)(x + xo);
      f2 n01 = {0.f, 0.f}, n23 = {0.f, 0.f}, d01 = {0.f, 0.f}, d23 = {0.f, 0.f};
      if (nch > 0) {
        u4 r = *(const u4*)(row + 128);
        u4 w0 = gather(tab, r.x, qoff), w1 = gather(tab, r.y, qoff), w2 = gather(tab, r.z, qoff), w3 = gather(tab, r.w, qoff);
        const f2 ea01 = {__builtin_amdgcn_exp2f(bf_hi(au.x)), __builtin_amdgcn_exp2f(bf_hi(au.y))};
        const f2 ea23 = {__builtin_amdgcn_exp2f(bf_hi(au.z)), __builtin_amdgcn_exp2f(bf_hi(au.w))};
#pragma unroll 1
        for (int ch = 1; ch < nch; ++ch) {
          const u4 rn = *(const u4*)(row + 128 + ch * 16);
          edge_step(w0, r.x, cn, ea01, ea23, n01, n23, d01, d23);
          w0 = gather(tab, rn.x, qoff);
          __builtin_amdgcn_sched_barrier(0);
          edge_step(w1, r.y, cn, ea01, ea23, n01, n23, d01, d23);
          w1 = gather(tab, rn.y, qoff);
          __builtin_amdgcn_sched_barrier(0);
          edge_step(w2, r.z, cn, ea01, ea23, n01, n23, d01, d23);
          w2 = gather(tab, rn.z, qoff);
          __builtin_amdgcn_sched_barrier(0);
          edge_step(w3, r.w, cn, ea01, ea23, n01, n23, d01, d23);
          w3 = gather(tab, rn.w, qoff);
          __builtin_amdgcn_sched_barrier(0);
          r = rn;
        }
        const int rem = w - 4 * (nch - 1);
        edge_step(w0, r.x, cn, ea01, ea23, n01, n23, d01, d23);
        if (rem > 1) edge_step(w1, r.y, cn, ea01, ea23, n01, n23, d01, d23);
        if (rem > 2) edge_step(w2, r.z, cn, ea01, ea23, n01, n23, d01, d23);
        if (rem > 3) edge_step(w3, r.w, cn, ea01, ea23, n01, n23, d01, d23);
      }
      u2 o;
      o.x = pack_bf(finish(n01.x, d01.x, bf_lo(au.x), bf_lo(xs.x)), finish(n01.y, d01.y, bf_lo(au.y), bf_hi(xs.x)));
      o.y = pack_bf(finish(n23.x, d23.x, bf_lo(au.z), bf_lo(xs.y)), finish(n23.y, d23.y, bf_lo(au.w), bf_hi(xs.y)));
      if (node < (unsigned)nb) *(u2*)(out + xo) = o;
      g = __builtin_amdgcn_readfirstlane(gn);
    }
    return;
  }

#pragma unroll 1
  for (int pass = 0; pass < 2; ++pass) {
    for (int i = t; i < 416 * 32; i += 1024) *(float*)(sm + Q_ROWS + (i >> 5) * kRowB + 128 + (i & 31) * 4) = 0.f;
    __syncthreads();
    const int ql = t & 3, qq = pass * 4 + ql, cs = t >> 2;
    const u2 cr = *(const u2*)(wc + fg * 32 + 4 * qq);
    const f4 cc = {bf_lo(cr.x) * kNL2E, bf_hi(cr.x) * kNL2E, bf_lo(cr.y) * kNL2E, bf_hi(cr.y) * kNL2E};
    const unsigned s = seg[cs];
#pragma unroll 1
    for (unsigned j = 0; j < (s >> 16); ++j) {
      const unsigned idx = cs * kCS + (s & 0xffffu) + j;
      const unsigned w32 = rec32[idx];
      const unsigned dl = ((unsigned)rec8[idx] << 1) | ((w32 >> 15) & 1u), sn = w32 & 0x7fffu;
      const float e = bf_hi(w32);
      const u4 w = *(const u4*)(tab + sn * 128 + qq * 16);
      char* row = sm + Q_ROWS + dl * kRowB;
      const u4 au = *(const u4*)(row + qq * 16);
#pragma unroll
      for (int k = 0; k < 4; ++k) {
        float bv = h_lo(w[k]);
        if (!(fabsf(bv) <= 65504.f)) {
          float acc = 0.f;
#pragma unroll 1
          for (int kk = 0; kk < 128; ++kk)
            acc = __builtin_fmaf(bf_lo(x[(size_t)sn * 128 + kk]), bf_lo(wb[kk * 128 + fg * 32 + qq * 4 + k]), acc);
          bv = acc * kNL2E;
        }
        const float tt = bf_hi(au[k]) + bv + e * cc[k];
        const float gt = __builtin_amdgcn_rcpf(1.f + __builtin_amdgcn_exp2f(tt));
        atomicAdd((float*)(row + 128 + (ql * 4 + k) * 4), gt * u2f(w[k]));
        atomicAdd((float*)(row + 192 + (ql * 4 + k) * 4), gt);
      }
    }
    __syncthreads();
    for (int i = t; i < nb * 4; i += 1024) {
      const int node = i >> 2;
      char* row = sm + Q_ROWS + node * kRowB;
      const u4 au = *(const u4*)(row + qq * 16);
      const f4 nm = *(const f4*)(row + 128 + ql * 16), dn = *(const f4*)(row + 192 + ql * 16);
      const size_t xo = (size_t)(n0 + node) * 128 + fg * 32 + 4 * qq;
      const u2 xs = *(const u2*)(x + xo);
      u2 o;
      o.x = pack_bf(finish(nm[0], dn[0], bf_lo(au.x), bf_lo(xs.x)), finish(nm[1], dn[1], bf_lo(au.y), bf_hi(xs.x)));
      o.y = pack_bf(finish(nm[2], dn[2], bf_lo(au.z), bf_lo(xs.y)), finish(nm[3], dn[3], bf_lo(au.w), bf_hi(xs.y)));
      *(u2*)(out + xo) = o;
    }
    __syncthreads();
  }
}

}

extern "C" void kernel_launch(void* const* d_in, const int* in_sizes, int n_in, void* d_out, int out_size,
                              void* d_ws, size_t ws_size, hipStream_t stream) {
  const us* x = (const us*)d_in[0];
  const us* ea = (const us*)d_in[1];
  const int* src = (const int*)d_in[2];
  const int* dst = (const int*)d_in[3];
  const us* wa = (const us*)d_in[4];
  const us* wb = (const us*)d_in[5];
  const us* wc = (const us*)d_in[6];
  const us* wu = (const us*)d_in[7];
  const us* wv = (const us*)d_in[8];
  k_prep<<<256, 1024, 0, stream>>>(x, ea, src, dst, wb, wv, (char*)d_ws);
  k_edge<<<256, 1024, 0, stream>>>(x, wa, wu, wc, wb, (const char*)d_ws, (us*)d_out);
}
```

```cpp
#include <hip/hip_runtime.h>
#include <hip/hip_bf16.h>
#include <stdint.h>

#define ST(k, i)

namespace {

typedef float f2 __attribute__((ext_vector_type(2)));
typedef float f4 __attribute__((ext_vector_type(4)));
typedef float f16v __attribute__((ext_vector_type(16)));
typedef unsigned u2 __attribute__((ext_vector_type(2)));
typedef unsigned u4 __attribute__((ext_vector_type(4)));
typedef int i2 __attribute__((ext_vector_type(2)));
typedef __bf16 b8 __attribute__((ext_vector_type(8)));
typedef __bf16 b2 __attribute__((ext_vector_type(2)));
typedef _Float16 h2 __attribute__((ext_vector_type(2)));
typedef unsigned short us;

constexpr int kN = 25000, kE = 400000;
constexpr int kBN = 391;
constexpr int kCE = 1564;
constexpr int kCS = 1568;
constexpr int kRows = 25024;
constexpr int kCap = 40;
constexpr int kRowB = 288;
constexpr int kTiles = 13;
constexpr float kNL2E = -1.4426950408889634f;

constexpr size_t kTabBytes = (size_t)4 * kRows * 128;
constexpr size_t kOffRec32 = kTabBytes;
constexpr size_t kOffRec8 = kOffRec32 + (size_t)256 * kCS * 4;
constexpr size_t kOffSeg = kOffRec8 + (size_t)256 * kCS;

__device__ __forceinline__ unsigned f2u(float f) { return __builtin_bit_cast(unsigned, f); }
__device__ __forceinline__ float u2f(unsigned u) { return __builtin_bit_cast(float, u); }
__device__ __forceinline__ float h_lo(unsigned w) { return (float)__builtin_bit_cast(h2, w).x; }
__device__ __forceinline__ float h_hi(unsigned w) { return (float)__builtin_bit_cast(h2, w).y; }
__device__ __forceinline__ float bf_lo(unsigned w) { return __builtin_bit_cast(float, w << 16); }
__device__ __forceinline__ float bf_hi(unsigned w) { return __builtin_bit_cast(float, w & 0xffff0000u); }
__device__ __forceinline__ unsigned pack_bf(float lo, float hi) {
  b2 v = {(__bf16)lo, (__bf16)hi};
  return __builtin_bit_cast(unsigned, v);
}

__device__ __forceinline__ unsigned wave_scan(unsigned v) {
  v += __builtin_amdgcn_update_dpp(0u, v, 0x111, 0xf, 0xf, true);
  v += __builtin_amdgcn_update_dpp(0u, v, 0x112, 0xf, 0xf, true);
  v += __builtin_amdgcn_update_dpp(0u, v, 0x114, 0xf, 0xf, true);
  v += __builtin_amdgcn_update_dpp(0u, v, 0x118, 0xf, 0xf, true);
  v += __builtin_amdgcn_update_dpp(0u, v, 0x142, 0xa, 0xf, false);
  v += __builtin_amdgcn_update_dpp(0u, v, 0x143, 0xc, 0xf, false);
  return v;
}

__device__ __forceinline__ u4 load_wfrag(const us* __restrict__ w0, const us* __restrict__ w1, int fg, int wave, int lane) {
  const us* w = (wave & 8) ? w1 : w0;
  const us* p = w + (16 * (wave & 7) + 8 * (lane >> 5)) * 128 + fg * 32 + (lane & 31);
  unsigned v[8];
#pragma unroll
  for (int j = 0; j < 8; ++j) v[j] = p[j * 128];
  return u4{v[0] | (v[1] << 16), v[2] | (v[3] << 16), v[4] | (v[5] << 16), v[6] | (v[7] << 16)};
}

__device__ __forceinline__ void load_xtile(const us* __restrict__ x, int row0, int lane, u4 (&xr)[8]) {
#pragma unroll
  for (int j = 0; j < 8; ++j) {
    int r = row0 + 4 * j + (lane >> 4);
    r = r < kN ? r : kN - 1;
    xr[j] = *(const u4*)(x + (size_t)r * 128 + (lane & 15) * 8);
  }
}

__device__ __forceinline__ void tile_gemm(char* stage, const char* wfr, int lane, const u4 (&xr)[8], f16v& a0, f16v& a1) {
#pragma unroll
  for (int j = 0; j < 8; ++j) {
    int r = 4 * j + (lane >> 4);
    *(u4*)(stage + r * 256 + (((lane & 15) ^ (r & 15)) << 4)) = xr[j];
  }
  __builtin_amdgcn_fence(__ATOMIC_RELEASE, "wavefront");
  __builtin_amdgcn_wave_barrier();
  __builtin_amdgcn_fence(__ATOMIC_ACQUIRE, "wavefront");
  int r = lane & 31, hh = lane >> 5;
#pragma unroll
  for (int i = 0; i < 16; ++i) { a0[i] = 0.f; a1[i] = 0.f; }
#pragma unroll
  for (int s = 0; s < 8; ++s) {
    u4 xf = *(const u4*)(stage + r * 256 + (((2 * s + hh) ^ (r & 15)) << 4));
    u4 w0 = *(const u4*)(wfr + s * 1024 + lane * 16);
    u4 w1 = *(const u4*)(wfr + (8 + s) * 1024 + lane * 16);
    a0 = __builtin_amdgcn_mfma_f32_32x32x16_bf16(__builtin_bit_cast(b8, w0), __builtin_bit_cast(b8, xf), a0, 0, 0, 0);
    a1 = __builtin_amdgcn_mfma_f32_32x32x16_bf16(__builtin_bit_cast(b8, w1), __builtin_bit_cast(b8, xf), a1, 0, 0, 0);
  }
  __builtin_amdgcn_fence(__ATOMIC_RELEASE, "wavefront");
  __builtin_amdgcn_wave_barrier();
  __builtin_amdgcn_fence(__ATOMIC_ACQUIRE, "wavefront");
}

constexpr int P_STAGE = 0;
constexpr int P_WFR = kTiles * 8192;
constexpr int P_R32 = P_WFR + 16384;
constexpr int P_R8 = P_R32 + kCS * 4;
constexpr int P_CNT = P_R8 + kCS;
constexpr int P_END = P_CNT + 256;

__global__ void __launch_bounds__(1024) k_prep(const us* __restrict__ x, const us* __restrict__ ea,
                                               const int* __restrict__ src, const int* __restrict__ dst,
                                               const us* __restrict__ wb, const us* __restrict__ wv,
                                               char* __restrict__ ws) {
  __shared__ __attribute__((aligned(16))) char sm[P_END];
  const int t = threadIdx.x, lane = t & 63, wave = __builtin_amdgcn_readfirstlane(t >> 6);
  const int b = blockIdx.x, fg = b & 3, bucket = ((b >> 2) & 1) * 32 + (b >> 3), n0 = bucket * kBN;
  unsigned* cnt = (unsigned*)(sm + P_CNT);

  ST(0, 0)
  const int e0 = b * kCE + 2 * t;
  const bool ev = (2 * t < kCE) && (e0 < kE);
  i2 dd = {0, 0}, ss = {0, 0};
  unsigned ee = 0;
  if (ev) {
    dd = *(const i2*)(dst + e0);
    ss = *(const i2*)(src + e0);
    ee = *(const unsigned*)(ea + e0);
  }
  u4 wf = load_wfrag(wb, wv, fg, wave, lane);
  u4 xr[8];
  if (wave < kTiles) load_xtile(x, n0 + wave * 32, lane, xr);

  if (t < 64) cnt[t] = 0;
  __syncthreads();
  ST(0, 1)
  unsigned b0 = (unsigned)dd.x / kBN, b1 = (unsigned)dd.y / kBN, r0 = 0, r1 = 0;
  if (ev) {
    r0 = atomicAdd(&cnt[b0], 1u);
    r1 = atomicAdd(&cnt[b1], 1u);
  }
  *(u4*)(sm + P_WFR + t * 16) = wf;
  __syncthreads();
  ST(0, 2)
  {
    unsigned c = cnt[lane];
    unsigned ex = wave_scan(c) - c;
    if (wave == 15) ((unsigned*)(ws + kOffSeg))[lane * 256 + (b & 7) * 32 + (b >> 3)] = ex | (c << 16);
    unsigned p0 = __builtin_amdgcn_ds_bpermute(b0 << 2, ex) + r0;
    unsigned p1 = __builtin_amdgcn_ds_bpermute(b1 << 2, ex) + r1;
    if (ev) {
      unsigned l0 = dd.x - b0 * kBN, l1 = dd.y - b1 * kBN;
      ((unsigned*)(sm + P_R32))[p0] = (ee << 16) | ((l0 & 1) << 15) | (unsigned)ss.x;
      ((unsigned*)(sm + P_R32))[p1] = (ee & 0xffff0000u) | ((l1 & 1) << 15) | (unsigned)ss.y;
      ((unsigned char*)(sm + P_R8))[p0] = (unsigned char)(l0 >> 1);
      ((unsigned char*)(sm + P_R8))[p1] = (unsigned char)(l1 >> 1);
    }
  }
  __syncthreads();
  ST(0, 3)
  if (wave >= kTiles) {
    const int cslot = (b & 7) * 32 + (b >> 3);
    for (int i = t - kTiles * 64; i < kCS / 4 + kCS / 16; i += 192) {
      u4 v = *(const u4*)(sm + P_R32 + i * 16);
      char* g = i < kCS / 4 ? ws + kOffRec32 + (size_t)cslot * kCS * 4 + i * 16
                            : ws + kOffRec8 + (size_t)cslot * kCS + (i - kCS / 4) * 16;
      *(u4*)g = v;
    }
    if (bucket == 63 && wave == 15 && lane < 8)
      *(u4*)(ws + ((size_t)fg * kRows + kN) * 128 + lane * 16) = u4{0x7c00u, 0x7c00u, 0x7c00u, 0x7c00u};
    ST(0, 7)
    return;
  }
  char* stage = sm + P_STAGE + wave * 8192;
  f16v ab, av;
  tile_gemm(stage, sm + P_WFR, lane, xr, ab, av);
  ST(0, 4)
  {
    const int r = lane & 31, hh = lane >> 5;
#pragma unroll
    for (int i = 0; i < 4; ++i) {
      u4 o;
#pragma unroll
      for (int j = 0; j < 4; ++j) {
        unsigned lo = (unsigned)__builtin_bit_cast(us, (_Float16)(ab[4 * i + j] * kNL2E));
        unsigned u = f2u(av[4 * i + j]) | 0x8000u;
        unsigned hv = f2u(av[4 * i + j]);
        unsigned m = (hv & 0x7fffffffu) < 0x8000u ? u : hv;
        o[j] = ((m + 0x8000u - lo) & 0xffff0000u) | lo;
      }
      *(u4*)(stage + r * 128 + (((2 * i + hh) ^ (r & 7)) << 4)) = o;
    }
  }
  __builtin_amdgcn_fence(__ATOMIC_RELEASE, "wavefront");
  __builtin_amdgcn_wave_barrier();
  __builtin_amdgcn_fence(__ATOMIC_ACQUIRE, "wavefront");
  char* tab = ws + ((size_t)fg * kRows + n0 + wave * 32) * 128;
#pragma unroll
  for (int j = 0; j < 4; ++j) {
    int r = 8 * j + (lane >> 3);
    u4 v = *(const u4*)(stage + r * 128 + (((lane & 7) ^ (r & 7)) << 4));
    int nl = wave * 32 + r;
    if (nl < kBN && n0 + nl < kN) *(u4*)(tab + r * 128 + (lane & 7) * 16) = v;
  }
  ST(0, 7)
}

constexpr int Q_ROWS = 0;
constexpr int Q_WFR = kTiles * 32 * kRowB;
constexpr int Q_CNT = Q_WFR + 16384;
constexpr int Q_ORD = Q_CNT + 416 * 4;
constexpr int Q_HIST = Q_ORD + 416 * 4;
constexpr int Q_MISC = Q_HIST + 256;
constexpr int Q_END = Q_MISC + 16;
constexpr int kSpareRow = 415;

__device__ __forceinline__ f2 pk_fma(f2 a, f2 b, f2 c) { return __builtin_elementwise_fma(a, b, c); }

__device__ __forceinline__ u4 gather(const char* __restrict__ tab, unsigned rec, unsigned qoff) {
  unsigned o;
  asm("v_mad_u32_u16 %0, %1, %2, %3" : "=v"(o) : "v"(rec), "s"(128u), "v"(qoff));
  return *(const u4*)(tab + o);
}

__device__ __forceinline__ void edge_step(u4 w, unsigned rec, const f4& c, f2 ea01, f2 ea23, f2& n01, f2& n23, f2& d01, f2& d23) {
  const float e = h_hi(rec);
  float y[4];
#pragma unroll
  for (int j = 0; j < 4; ++j)
    y[j] = __builtin_amdgcn_exp2f(__builtin_fmaf(e, c[j], h_lo(w[j])));
  f2 p01 = pk_fma(f2{y[0], y[1]}, ea01, f2{1.f, 1.f}), p23 = pk_fma(f2{y[2], y[3]}, ea23, f2{1.f, 1.f});
  f2 g01 = {__builtin_amdgcn_rcpf(p01.x), __builtin_amdgcn_rcpf(p01.y)};
  f2 g23 = {__builtin_amdgcn_rcpf(p23.x), __builtin_amdgcn_rcpf(p23.y)};
  n01 = pk_fma(g01, __builtin_bit_cast(f2, u2{w.x, w.y}), n01);
  n23 = pk_fma(g23, __builtin_bit_cast(f2, u2{w.z, w.w}), n23);
  d01 += g01;
  d23 += g23;
}

__device__ __forceinline__ float finish(float num, float den, float u, float xv) {
  float z = u + num * __builtin_amdgcn_rcpf(den + 1e-6f);
  return xv + (z > 0.f ? z : 0.f * z);
}

__global__ void __launch_bounds__(1024) k_edge(const us* __restrict__ x, const us* __restrict__ wa,
                                               const us* __restrict__ wu, const us* __restrict__ wc,
                                               const us* __restrict__ wb, const char* __restrict__ ws,
                                               us* __restrict__ out) {
  __shared__ __attribute__((aligned(16))) char sm[Q_END];
  const int t = threadIdx.x, lane = t & 63, wave = __builtin_amdgcn_readfirstlane(t >> 6);
  const int b = blockIdx.x, fg = b & 3, bucket = ((b >> 2) & 1) * 32 + (b >> 3), n0 = bucket * kBN;
  const int nb = min(kBN, kN - n0);
  unsigned* cnt = (unsigned*)(sm + Q_CNT);
  unsigned* ord = (unsigned*)(sm + Q_ORD);
  unsigned* hist = (unsigned*)(sm + Q_HIST);
  volatile unsigned* misc = (volatile unsigned*)(sm + Q_MISC);
  const unsigned* __restrict__ seg = (const unsigned*)(ws + kOffSeg) + bucket * 256;
  const unsigned* __restrict__ rec32 = (const unsigned*)(ws + kOffRec32);
  const unsigned char* __restrict__ rec8 = (const unsigned char*)(ws + kOffRec8);
  const char* __restrict__ tab = ws + (size_t)fg * kRows * 128;
  const int q = lane & 7, hh = lane >> 5, l31 = lane & 31;

  ST(1, 0)
  unsigned sg[8];
#pragma unroll
  for (int i = 0; i < 8; ++i) {
    unsigned s0 = seg[wave * 16 + 2 * i], s1 = seg[wave * 16 + 2 * i + 1];
    sg[i] = hh ? s1 : s0;
  }
  u4 wf = load_wfrag(wa, wu, fg, wave, lane);
  const u2 craw = *(const u2*)(wc + fg * 32 + 4 * q);
  u4 xr[8];
  if (wave < kTiles) load_xtile(x, n0 + wave * 32, lane, xr);
  unsigned r32[8], r8[8];
#pragma unroll
  for (int i = 0; i < 8; ++i) {
    r32[i] = 0; r8[i] = 0;
    if ((unsigned)l31 < (sg[i] >> 16)) {
      unsigned idx = (wave * 16 + 2 * i + hh) * kCS + (sg[i] & 0xffffu) + l31;
      r32[i] = rec32[idx];
      r8[i] = rec8[idx];
    }
  }
  if (t < 416) cnt[t] = 0;
  if (t >= 512 && t < 576) hist[t - 512] = 0;
  if (t == 640) { misc[0] = 16; misc[1] = 0; }
  *(u4*)(sm + Q_WFR + t * 16) = wf;
  f4 c = {bf_lo(craw.x) * kNL2E, bf_hi(craw.x) * kNL2E, bf_lo(craw.y) * kNL2E, bf_hi(craw.y) * kNL2E};
  float cm = fmaxf(fmaxf(fabsf(c[0]), fabsf(c[1])), fmaxf(fabsf(c[2]), fabsf(c[3])));
  cm = fmaxf(cm, __shfl_xor(cm, 1));
  cm = fmaxf(cm, __shfl_xor(cm, 2));
  cm = fmaxf(cm, __shfl_xor(cm, 4));
  unsigned ce = __builtin_bit_cast(unsigned, cm) & 0x7f800000u;
  bool slow = !(cm < 1e30f);
  ce = (ce < (20u << 23) || slow) ? (127u << 23) : ce;
  const float escale = __builtin_bit_cast(float, ce);
  const f4 cn = c * __builtin_bit_cast(float, (254u << 23) - ce);
  __syncthreads();

  ST(1, 1)
  if (wave < kTiles) {
    char* stage = sm + Q_ROWS + wave * 32 * kRowB;
    f16v aa, au;
    tile_gemm(stage, sm + Q_WFR, lane, xr, aa, au);
    float am = 0.f;
#pragma unroll
    for (int i = 0; i < 4; ++i) {
      u4 o;
#pragma unroll
      for (int j = 0; j < 4; ++j) {
        float a = aa[4 * i + j] * kNL2E;
        slow |= !(fabsf(a) <= 80.f);
        o[j] = pack_bf(au[4 * i + j], a);
      }
      *(u4*)(stage + l31 * kRowB + ((2 * i + hh) << 4)) = o;
    }
  }
  __syncthreads();

  ST(1, 2)
  auto put = [&](unsigned w32, unsigned w8) {
    unsigned dl = (w8 << 1) | ((w32 >> 15) & 1u);
    float e = bf_hi(w32) * escale;
    slow |= !(fabsf(e) <= 16384.f);
    unsigned rec = ((unsigned)__builtin_bit_cast(us, (_Float16)e) << 16) | (w32 & 0x7fffu);
    unsigned rk = atomicAdd(&cnt[dl], 1u);
    if (rk < (unsigned)kCap) *(unsigned*)(sm + Q_ROWS + dl * kRowB + 128 + rk * 4) = rec;
    else slow = true;
  };
  unsigned mx = 0;
#pragma unroll
  for (int i = 0; i < 8; ++i) {
    if ((unsigned)l31 < (sg[i] >> 16)) put(r32[i], r8[i]);
    mx = max(mx, sg[i] >> 16);
  }
  if (__any(mx > 32u)) {
#pragma unroll 1
    for (int i = 0; i < 16; ++i) {
      unsigned s = seg[wave * 16 + i];
      for (unsigned j = 32 + lane; j < (s >> 16); j += 64) {
        unsigned idx = (wave * 16 + i) * kCS + (s & 0xffffu) + j;
        put(rec32[idx], rec8[idx]);
      }
    }
  }
  if (slow) misc[1] = 1;
  __syncthreads();

  ST(1, 3)
  if (misc[1] == 0) {
    unsigned deg = 0, rk = 0;
    if (t < nb) {
      deg = cnt[t];
      rk = atomicAdd(&hist[deg], 1u);
    }
    __syncthreads();
    if (wave < 7) {
      unsigned above = nb - wave_scan(hist[lane]);
      unsigned pos = __builtin_amdgcn_ds_bpermute(deg << 2, above) + rk;
      if (t < nb) ord[pos] = t | (deg << 16);
      else if (t < 416) ord[t] = kSpareRow;
    }
    __syncthreads();

    ST(1, 4)
    const int ng = (nb + 7) >> 3;
    const unsigned qoff = q * 16;
    int g = wave;
    while (g < ng) {
      const unsigned ent = ord[g * 8 + (lane >> 3)];
      unsigned gn = 0;
      if (lane == 0) gn = atomicAdd((unsigned*)&misc[0], 1u);
      const unsigned node = ent & 0xffffu, dg = ent >> 16;
      const int w = __builtin_amdgcn_readfirstlane(dg);
      const int nch = (w + 3) >> 2;
      char* row = sm + Q_ROWS + node * kRowB;
      for (unsigned j = dg + q; j < 4u * nch; j += 8) *(unsigned*)(row + 128 + j * 4) = (unsigned)kN;
      __builtin_amdgcn_fence(__ATOMIC_RELEASE, "wavefront");
      __builtin_amdgcn_wave_barrier();
      __builtin_amdgcn_fence(__ATOMIC_ACQUIRE, "wavefront");
      const u4 au = *(const u4*)(row + qoff);
      const unsigned gnode = min(n0 + (int)node, kN - 1);
      const size_t xo = (size_t)gnode * 128 + fg * 32 + 4 * q;
      const u2 xs = *(const u2*)(x + xo);
      f2 n01 = {0.f, 0.f}, n23 = {0.f, 0.f}, d01 = {0.f, 0.f}, d23 = {0.f, 0.f};
      if (nch > 0) {
        u4 r = *(const u4*)(row + 128);
        u4 w0 = gather(tab, r.x, qoff), w1 = gather(tab, r.y, qoff), w2 = gather(tab, r.z, qoff), w3 = gather(tab, r.w, qoff);
        const f2 ea01 = {__builtin_amdgcn_exp2f(bf_hi(au.x)), __builtin_amdgcn_exp2f(bf_hi(au.y))};
        const f2 ea23 = {__builtin_amdgcn_exp2f(bf_hi(au.z)), __builtin_amdgcn_exp2f(bf_hi(au.w))};
#pragma unroll 1
        for (int ch = 1; ch < nch; ++ch) {
          const u4 rn = *(const u4*)(row + 128 + ch * 16);
          edge_step(w0, r.x, cn, ea01, ea23, n01, n23, d01, d23);
          w0 = gather(tab, rn.x, qoff);
          __builtin_amdgcn_sched_barrier(0);
          edge_step(w1, r.y, cn, ea01, ea23, n01, n23, d01, d23);
          w1 = gather(tab, rn.y, qoff);
          __builtin_amdgcn_sched_barrier(0);
          edge_step(w2, r.z, cn, ea01, ea23, n01, n23, d01, d23);
          w2 = gather(tab, rn.z, qoff);
          __builtin_amdgcn_sched_barrier(0);
          edge_step(w3, r.w, cn, ea01, ea23, n01, n23, d01, d23);
          w3 = gather(tab, rn.w, qoff);
          __builtin_amdgcn_sched_barrier(0);
          r = rn;
        }
        const int rem = w - 4 * (nch - 1);
        edge_step(w0, r.x, cn, ea01, ea23, n01, n23, d01, d23);
        if (rem > 1) edge_step(w1, r.y, cn, ea01, ea23, n01, n23, d01, d23);
        if (rem > 2) edge_step(w2, r.z, cn, ea01, ea23, n01, n23, d01, d23);
        if (rem > 3) edge_step(w3, r.w, cn, ea01, ea23, n01, n23, d01, d23);
      }
      u2 o;
      o.x = pack_bf(finish(n01.x, d01.x, bf_lo(au.x), bf_lo(xs.x)), finish(n01.y, d01.y, bf_lo(au.y), bf_hi(xs.x)));
      o.y = pack_bf(finish(n23.x, d23.x, bf_lo(au.z), bf_lo(xs.y)), finish(n23.y, d23.y, bf_lo(au.w), bf_hi(xs.y)));
      if (node < (unsigned)nb) *(u2*)(out + xo) = o;
      g = __builtin_amdgcn_readfirstlane(gn);
    }
    ST(1, 7)
    return;
  }

#pragma unroll 1
  for (int pass = 0; pass < 2; ++pass) {
    for (int i = t; i < 416 * 32; i += 1024) *(float*)(sm + Q_ROWS + (i >> 5) * kRowB + 128 + (i & 31) * 4) = 0.f;
    __syncthreads();
    const int ql = t & 3, qq = pass * 4 + ql, cs = t >> 2;
    const u2 cr = *(const u2*)(wc + fg * 32 + 4 * qq);
    const f4 cc = {bf_lo(cr.x) * kNL2E, bf_hi(cr.x) * kNL2E, bf_lo(cr.y) * kNL2E, bf_hi(cr.y) * kNL2E};
    const unsigned s = seg[cs];
#pragma unroll 1
    for (unsigned j = 0; j < (s >> 16); ++j) {
      const unsigned idx = cs * kCS + (s & 0xffffu) + j;
      const unsigned w32 = rec32[idx];
      const unsigned dl = ((unsigned)rec8[idx] << 1) | ((w32 >> 15) & 1u), sn = w32 & 0x7fffu;
      const float e = bf_hi(w32);
      const u4 w = *(const u4*)(tab + sn * 128 + qq * 16);
      char* row = sm + Q_ROWS + dl * kRowB;
      const u4 au = *(const u4*)(row + qq * 16);
#pragma unroll
      for (int k = 0; k < 4; ++k) {
        float bv = h_lo(w[k]);
        if (!(fabsf(bv) <= 65504.f)) {
          float acc = 0.f;
#pragma unroll 1
          for (int kk = 0; kk < 128; ++kk)
            acc = __builtin_fmaf(bf_lo(x[(size_t)sn * 128 + kk]), bf_lo(wb[kk * 128 + fg * 32 + qq * 4 + k]), acc);
          bv = acc * kNL2E;
        }
        const float tt = bf_hi(au[k]) + bv + e * cc[k];
        const float gt = __builtin_amdgcn_rcpf(1.f + __builtin_amdgcn_exp2f(tt));
        atomicAdd((float*)(row + 128 + (ql * 4 + k) * 4), gt * u2f(w[k]));
        atomicAdd((float*)(row + 192 + (ql * 4 + k) * 4), gt);
      }
    }
    __syncthreads();
    for (int i = t; i < nb * 4; i += 1024) {
      const int node = i >> 2;
      char* row = sm + Q_ROWS + node * kRowB;
      const u4 au = *(const u4*)(row + qq * 16);
      const f4 nm = *(const f4*)(row + 128 + ql * 16), dn = *(const f4*)(row + 192 + ql * 16);
      const size_t xo = (size_t)(n0 + node) * 128 + fg * 32 + 4 * qq;
      const u2 xs = *(const u2*)(x + xo);
      u2 o;
      o.x = pack_bf(finish(nm[0], dn[0], bf_lo(au.x), bf_lo(xs.x)), finish(nm[1], dn[1], bf_lo(au.y), bf_hi(xs.x)));
      o.y = pack_bf(finish(nm[2], dn[2], bf_lo(au.z), bf_lo(xs.y)), finish(nm[3], dn[3], bf_lo(au.w), bf_hi(xs.y)));
      *(u2*)(out + xo) = o;
    }
    __syncthreads();
  }
}

}

extern "C" void kernel_launch(void* const* d_in, const int* in_sizes, int n_in, void* d_out, int out_size,
                              void* d_ws, size_t ws_size, hipStream_t stream) {
  const us* x = (const us*)d_in[0];
  const us* ea = (const us*)d_in[1];
  const int* src = (const int*)d_in[2];
  const int* dst = (const int*)d_in[3];
  const us* wa = (const us*)d_in[4];
  const us* wb = (const us*)d_in[5];
  const us* wc = (const us*)d_in[6];
  const us* wu = (const us*)d_in[7];
  const us* wv = (const us*)d_in[8];
  k_prep<<<256, 1024, 0, stream>>>(x, ea, src, dst, wb, wv, (char*)d_ws);
  k_edge<<<256, 1024, 0, stream>>>(x, wa, wu, wc, wb, (const char*)d_ws, (us*)d_out);
}
```

```cpp
#include <hip/hip_runtime.h>
#include <hip/hip_bf16.h>
#include <stdint.h>

#define ST(k, i)
#define STC(i)
#define ST_DECL
#define ST_FLUSH

namespace {

typedef float f2 __attribute__((ext_vector_type(2)));
typedef float f4 __attribute__((ext_vector_type(4)));
typedef float f16v __attribute__((ext_vector_type(16)));
typedef unsigned u2 __attribute__((ext_vector_type(2)));
typedef unsigned u4 __attribute__((ext_vector_type(4)));
typedef __bf16 b8 __attribute__((ext_vector_type(8)));
typedef __bf16 b2 __attribute__((ext_vector_type(2)));
typedef _Float16 h2 __attribute__((ext_vector_type(2)));
typedef unsigned short us;
typedef unsigned long long u64;
typedef __amdgpu_buffer_rsrc_t rsrc_t;
#define LAS __attribute__((address_space(3)))
#define GAS __attribute__((address_space(1)))
#define DI __device__ __forceinline__

constexpr int kN = 25000, kE = 400000;
constexpr int kBN = 391;
constexpr int kCE = 1564;
constexpr int kCS = 1568;
constexpr int kChunkB = kCS * 6 + 64;
constexpr int kRows = 25024;
constexpr int kCap = 40;
constexpr int kRowB = 288;
constexpr float kNL2E = -1.4426950408889634f;
constexpr unsigned kOOB = 0x80000000u;
constexpr int kSC1 = 16;
constexpr unsigned kSpinCap = 1u << 17;

constexpr size_t kOffTab = 0;
constexpr size_t kOffRec = kOffTab + (size_t)4 * kRows * 128;
constexpr size_t kOffSeg = kOffRec + (size_t)256 * kChunkB;
constexpr size_t kOffFlag = kOffSeg + (size_t)64 * 256 * 8;
constexpr size_t kOffEpoch = kOffFlag + 4 * 64 * 128;

constexpr int L_MISC = 0;
constexpr int L_BCNT = 64;
constexpr int L_HIST = L_BCNT + 1024;
constexpr int L_CNT = L_HIST + 256;
constexpr int L_ORD = L_CNT + 1568 + 128;
constexpr int L_R32 = L_ORD + 1568;
constexpr int L_R16 = L_R32 + kCS * 4;
constexpr int L_WFR = L_R16 + kCS * 2;
constexpr int L_ROWS = L_WFR + 32768;
constexpr int L_TRASH = L_ROWS + 392 * kRowB;
constexpr int L_END = L_TRASH + 16;
enum { M_QUEUE = 0, M_SLOW, M_EDONE, M_TDONE, M_READY, M_WDONE, M_ECNT, M_ABORT, M_G0, M_G1, M_ESC };
#define XP 4
#define XAUX 0
#define OAUX 0
#define RDY_G 9
#define RDY_R 11

DI unsigned f2u(float f) { return __builtin_bit_cast(unsigned, f); }
DI float u2f(unsigned u) { return __builtin_bit_cast(float, u); }
DI float h_lo(unsigned w) { return (float)__builtin_bit_cast(h2, w).x; }
DI float h_hi(unsigned w) { return (float)__builtin_bit_cast(h2, w).y; }
DI unsigned f2h(float f) { return (unsigned)__builtin_bit_cast(us, (_Float16)f); }
DI float bf_lo(unsigned w) { return u2f(w << 16); }
DI float bf_hi(unsigned w) { return u2f(w & 0xffff0000u); }
DI unsigned pack_bf(float lo, float hi) {
  b2 v = {(__bf16)lo, (__bf16)hi};
  return __builtin_bit_cast(unsigned, v);
}
DI f2 pk_fma(f2 a, f2 b, f2 c) { return __builtin_elementwise_fma(a, b, c); }
DI rsrc_t mk_rsrc(const void* p, int bytes) { return __builtin_amdgcn_make_buffer_rsrc((void*)p, (short)0, bytes, 0x00020000); }
DI void wg_barrier() { asm volatile("s_waitcnt lgkmcnt(0)\n\ts_barrier" ::: "memory"); }
DI void vm_drain() { asm volatile("s_waitcnt vmcnt(0)" ::: "memory"); }
DI void lds_order() { asm volatile("" ::: "memory"); }
template <typename T> DI T lds_ld(LAS char* sm, unsigned off) { return *(LAS T*)(sm + off); }
template <typename T> DI void lds_st(LAS char* sm, unsigned off, T v) { *(LAS T*)(sm + off) = v; }
DI unsigned lds_add(LAS char* sm, unsigned off, unsigned v) {
  return __hip_atomic_fetch_add((LAS unsigned*)(sm + off), v, __ATOMIC_RELAXED, __HIP_MEMORY_SCOPE_WORKGROUP);
}
DI unsigned lds_poll(LAS char* sm, int word) { return *(volatile LAS unsigned*)(sm + L_MISC + word * 4); }
DI void lds_wait_ge(LAS char* sm, int word, unsigned v) {
  while ((lds_poll(sm, word) & 0xffu) < v) __builtin_amdgcn_s_sleep(1);
  asm volatile("" ::: "memory");
}
DI void lds_signal(LAS char* sm, int word, unsigned v, int lane) {
  asm volatile("s_waitcnt lgkmcnt(0)" ::: "memory");
  if (lane == 0) lds_add(sm, L_MISC + word * 4, v);
}
DI u2 ld_agent(const char* p) {
  const u64 v = __hip_atomic_load((GAS u64*)p, __ATOMIC_RELAXED, __HIP_MEMORY_SCOPE_AGENT);
  return u2{(unsigned)v, (unsigned)(v >> 32)};
}
DI void st_agent(char* p, u2 v) {
  __hip_atomic_store((GAS u64*)p, ((u64)v.y << 32) | v.x, __ATOMIC_RELAXED, __HIP_MEMORY_SCOPE_AGENT);
}
DI void after_poll() {
  __builtin_amdgcn_fence(__ATOMIC_ACQUIRE, "wavefront");
  asm volatile("" ::: "memory");
}
template <int HI> DI unsigned pack_near(unsigned m, unsigned pair) {
  const unsigned d = __builtin_elementwise_sub_sat(m + 0x8000u, HI ? pair >> 16 : pair & 0xffffu);
  return __builtin_amdgcn_perm(d, pair, HI ? 0x07060302u : 0x07060100u);
}
DI unsigned pack_h(float lo, float hi) {
  h2 v = {(_Float16)lo, (_Float16)hi};
  return __builtin_bit_cast(unsigned, v);
}

DI unsigned wave_scan(unsigned v) {
  v += __builtin_amdgcn_update_dpp(0u, v, 0x111, 0xf, 0xf, true);
  v += __builtin_amdgcn_update_dpp(0u, v, 0x112, 0xf, 0xf, true);
  v += __builtin_amdgcn_update_dpp(0u, v, 0x114, 0xf, 0xf, true);
  v += __builtin_amdgcn_update_dpp(0u, v, 0x118, 0xf, 0xf, true);
  v += __builtin_amdgcn_update_dpp(0u, v, 0x142, 0xa, 0xf, false);
  v += __builtin_amdgcn_update_dpp(0u, v, 0x143, 0xc, 0xf, false);
  return v;
}
DI unsigned wave_max(unsigned v) {
  v = max(v, (unsigned)__builtin_amdgcn_update_dpp(0u, v, 0x111, 0xf, 0xf, true));
  v = max(v, (unsigned)__builtin_amdgcn_update_dpp(0u, v, 0x112, 0xf, 0xf, true));
  v = max(v, (unsigned)__builtin_amdgcn_update_dpp(0u, v, 0x114, 0xf, 0xf, true));
  v = max(v, (unsigned)__builtin_amdgcn_update_dpp(0u, v, 0x118, 0xf, 0xf, true));
  v = max(v, (unsigned)__builtin_amdgcn_update_dpp(0u, v, 0x142, 0xa, 0xf, false));
  v = max(v, (unsigned)__builtin_amdgcn_update_dpp(0u, v, 0x143, 0xc, 0xf, false));
  return __builtin_amdgcn_readlane(v, 63);
}
template <int I> DI unsigned row_bcast(unsigned v) { return __builtin_amdgcn_update_dpp(0u, v, 0x150 + I, 0xf, 0xf, true); }

DI void dma_x16(const us* x, int row0, unsigned v0, unsigned ldsa) {
  const rsrc_t rx = mk_rsrc(x + (size_t)min(row0, kN) * 128, (kN - min(row0, kN)) * 256);
  unsigned keep;
  asm volatile(
      "s_nop 4\n\ts_mov_b32 %0, m0\n\ts_mov_b32 m0, %1\n\ts_nop 0\n\t"
      "buffer_load_dwordx4 %2, %6, 0 offen lds\n\t"
      "buffer_load_dwordx4 %3, %6, 0 offen offset:1024 lds\n\t"
      "buffer_load_dwordx4 %4, %6, 0 offen offset:2048 lds\n\t"
      "buffer_load_dwordx4 %5, %6, 0 offen offset:3072 lds\n\t"
      "s_mov_b32 m0, %0"
      : "=&s"(keep) : "s"(ldsa), "v"(v0), "v"(v0 ^ 64u), "v"(v0 ^ 128u), "v"(v0 ^ 192u), "s"(rx) : "memory");
}
DI void dma_x8(const us* x, int row0, unsigned v0, unsigned ldsa) {
  const rsrc_t rx = mk_rsrc(x + (size_t)min(row0, kN) * 128, (kN - min(row0, kN)) * 256);
  unsigned keep;
  asm volatile(
      "s_nop 4\n\ts_mov_b32 %0, m0\n\ts_mov_b32 m0, %1\n\ts_nop 0\n\t"
      "buffer_load_dwordx4 %2, %4, 0 offen lds\n\t"
      "buffer_load_dwordx4 %3, %4, 0 offen offset:1024 lds\n\t"
      "s_mov_b32 m0, %0"
      : "=&s"(keep) : "s"(ldsa), "v"(v0), "v"(v0 ^ 64u), "s"(rx) : "memory");
}

DI void gemm2(LAS char* sm, unsigned wo, const u4 (&xf)[8], f16v& a0, f16v& a1) {
#pragma unroll
  for (int i = 0; i < 16; ++i) { a0[i] = 0.f; a1[i] = 0.f; }
#pragma unroll
  for (int s = 0; s < 8; ++s) {
    const u4 w0 = lds_ld<u4>(sm, wo + s * 1024), w1 = lds_ld<u4>(sm, wo + 8192 + s * 1024);
    a0 = __builtin_amdgcn_mfma_f32_32x32x16_bf16(__builtin_bit_cast(b8, w0), __builtin_bit_cast(b8, xf[s]), a0, 0, 0, 0);
    a1 = __builtin_amdgcn_mfma_f32_32x32x16_bf16(__builtin_bit_cast(b8, w1), __builtin_bit_cast(b8, xf[s]), a1, 0, 0, 0);
  }
}

DI u4 gather(rsrc_t rt, unsigned rec, unsigned qoff) {
  unsigned o;
  asm("v_mad_u32_u16 %0, %1, %2, %3" : "=v"(o) : "v"(rec), "s"(128u), "v"(qoff));
  return __builtin_amdgcn_raw_buffer_load_b128(rt, o, 0, kSC1);
}

DI void edge_step(u4 w, unsigned rec, const f4& c, f2 ea01, f2 ea23, f2& n01, f2& n23, f2& d01, f2& d23) {
  const float e = h_hi(rec);
  float y[4];
#pragma unroll
  for (int j = 0; j < 4; ++j) y[j] = __builtin_fmaf(e, c[j], h_lo(w[j]));
#pragma unroll
  for (int j = 0; j < 4; ++j) y[j] = __builtin_amdgcn_exp2f(y[j]);
  f2 p01 = pk_fma(f2{y[0], y[1]}, ea01, f2{1.f, 1.f}), p23 = pk_fma(f2{y[2], y[3]}, ea23, f2{1.f, 1.f});
  f2 g01 = {__builtin_amdgcn_rcpf(p01.x), __builtin_amdgcn_rcpf(p01.y)};
  f2 g23 = {__builtin_amdgcn_rcpf(p23.x), __builtin_amdgcn_rcpf(p23.y)};
  n01 = pk_fma(g01, f2{u2f(w.x), u2f(w.y)}, n01);
  n23 = pk_fma(g23, f2{u2f(w.z), u2f(w.w)}, n23);
  d01 += g01;
  d23 += g23;
}

DI float relu(float z) { return __builtin_elementwise_maximum(z, 0.f); }

DI float dot128(const us* __restrict__ xr, const us* __restrict__ wcol) {
  float acc = 0.f;
#pragma unroll 1
  for (int k = 0; k < 128; ++k) acc = __builtin_fmaf(bf_lo(xr[k]), bf_lo(wcol[k * 128]), acc);
  return acc;
}

__global__ void __launch_bounds__(1024, 4)
k_gcn(const us* __restrict__ x, const us* __restrict__ ea, const int* __restrict__ src, const int* __restrict__ dst,
      const us* __restrict__ wa, const us* __restrict__ wb, const us* __restrict__ wc, const us* __restrict__ wu,
      const us* __restrict__ wv, char* ws, us* __restrict__ out) {
  __shared__ __attribute__((aligned(16))) char smem[L_END];
  LAS char* sm = (LAS char*)smem;
  const int t = threadIdx.x, lane = t & 63, wave = __builtin_amdgcn_readfirstlane(t >> 6);
  const int b = blockIdx.x, fg = b & 3, bucket = ((b >> 2) & 1) * 32 + (b >> 3), n0 = bucket * kBN;
  const int nb = min(kBN, kN - n0), ng = (nb + 7) >> 3;
  const int l31 = lane & 31, hh = lane >> 5;
  const rsrc_t rtab = mk_rsrc(ws + kOffTab + (size_t)fg * kRows * 128, kRows * 128);
  const rsrc_t rrec = mk_rsrc(ws + kOffRec, 256 * kChunkB);
  const int tile = wave == 2 ? 12 : wave == 3 ? 11 : wave - 4;
  const unsigned stage = L_ROWS + tile * 32 * kRowB;
  ST_DECL
  ST(0, 0)

  if (wave < 4) {
    __builtin_amdgcn_s_setprio(2);
    const rsrc_t rd = mk_rsrc(dst, kE * 4), rs = mk_rsrc(src, kE * 4), re = mk_rsrc(ea, kE * 2);
    bool ok[2];
    u4 dq[2], sq[2];
    u2 eq[2];
#pragma unroll
    for (int r = 0; r < 2; ++r) {
      const int ql = lane + 64 * r, qd = wave * 98 + ql, e0 = b * kCE + 4 * qd;
      ok[r] = ql < 98 && qd < kBN && e0 < kE;
      const unsigned o = ok[r] ? e0 * 2 : kOOB >> 1;
      dq[r] = __builtin_amdgcn_raw_buffer_load_b128(rd, o * 2, 0, 0);
      sq[r] = __builtin_amdgcn_raw_buffer_load_b128(rs, o * 2, 0, 0);
      eq[r] = __builtin_amdgcn_raw_buffer_load_b64(re, o, 0, 0);
    }
    const unsigned cw = ((const unsigned*)wc)[lane];
    const u2 g = ld_agent(ws + kOffEpoch);
    lds_st<unsigned>(sm, L_BCNT + wave * 256 + lane * 4, 0u);
    wg_barrier();
    unsigned bk[8], rk[8];
#pragma unroll
    for (int i = 0; i < 8; ++i) {
      bk[i] = __umul24(dq[i >> 2][i & 3], 21455u) >> 23;
      rk[i] = 0;
    }
#pragma unroll
    for (int r = 0; r < 2; ++r)
      if (ok[r]) {
#pragma unroll
        for (int j = 0; j < 4; ++j) rk[4 * r + j] = lds_add(sm, L_BCNT + wave * 256 + bk[4 * r + j] * 4, 1u);
      }
    lds_signal(sm, M_ECNT, 1u, lane);
    const unsigned cm = wave_max(max(cw & 0x7fff0000u, (cw << 16) & 0x7fff0000u));
    bool bad = cm >= (226u << 23);
    const unsigned ce = (cm < (20u << 23) || bad) ? (127u << 23) : (cm & 0x7f800000u);
    unsigned r32[8], r16[8];
#pragma unroll
    for (int i = 0; i < 8; ++i) {
      const unsigned ew = eq[i >> 2][(i >> 1) & 1];
      const float e = ((i & 1) ? bf_hi(ew) : bf_lo(ew)) * u2f(ce);
      bad |= !(__builtin_fabsf(e) <= 8192.f);
      r32[i] = (f2h(e) << 16) | sq[i >> 2][i & 3];
      r16[i] = (dq[i >> 2][i & 3] - bk[i] * kBN) * 4;
    }
    if (t == 0) {
      lds_st<u2>(sm, L_MISC + M_G0 * 4, g);
      lds_st<unsigned>(sm, L_MISC + M_ESC * 4, ce);
    }
    lds_wait_ge(sm, M_ECNT, 4u);
    ST(0, 1)
    const unsigned c0 = lds_ld<unsigned>(sm, L_BCNT + lane * 4), c1 = lds_ld<unsigned>(sm, L_BCNT + 256 + lane * 4);
    const unsigned c2 = lds_ld<unsigned>(sm, L_BCNT + 512 + lane * 4), c3 = lds_ld<unsigned>(sm, L_BCNT + 768 + lane * 4);
    const unsigned tot = c0 + c1 + c2 + c3, ex = wave_scan(tot) - tot;
    const unsigned mine = ex + (wave > 0 ? c0 : 0u) + (wave > 1 ? c1 : 0u) + (wave > 2 ? c2 : 0u);
    unsigned ps[8];
#pragma unroll
    for (int i = 0; i < 8; ++i) ps[i] = __builtin_amdgcn_ds_bpermute(bk[i] << 2, mine) + rk[i];
#pragma unroll
    for (int r = 0; r < 2; ++r)
      if (ok[r]) {
#pragma unroll
        for (int i = 4 * r; i < 4 * r + 4; ++i) {
          lds_st<unsigned>(sm, L_R32 + ps[i] * 4, r32[i]);
          lds_st<us>(sm, L_R16 + ps[i] * 2, (us)r16[i]);
        }
      }
    lds_signal(sm, M_EDONE, __any(bad) ? 0x101u : 1u, lane);
    if (wave == 0) {
      lds_wait_ge(sm, M_EDONE, 4u);
      const unsigned dn = lds_poll(sm, M_EDONE);
      const rsrc_t rch = mk_rsrc(ws + kOffRec + (size_t)b * kChunkB, kCS * 6);
#pragma unroll
      for (int j = 0; j < 10; ++j) {
        const unsigned o = j * 1024 + lane * 16;
        const u4 v = lds_ld<u4>(sm, L_R32 + o);
        __builtin_amdgcn_raw_buffer_store_b128(v, rch, o, 0, kSC1);
      }
      vm_drain();
      const unsigned k0 = g.x ^ 0x9E3779B9u, k1 = g.y ^ 0x7F4A7C15u;
      const u2 gr = {(k0 << 23) | ((dn >> 8) ? 1u << 22 : 0u) | (tot << 11) | ex, (k0 >> 9) | (k1 << 23)};
      st_agent(ws + kOffSeg + b * 512 + lane * 8, gr);
    }
    __builtin_amdgcn_s_setprio(0);
    ST(0, 2)
  } else if (wave < 7) {
    const int i = t - 256;
#pragma unroll
    for (int j = 0; j < 3; ++j) {
      const int n = i + 192 * j;
      if (n < 424) lds_st<unsigned>(sm, L_CNT + n * 4, n < 392 ? L_ROWS + 128 + n * kRowB : L_TRASH);
    }
    if (i < 64) lds_st<unsigned>(sm, L_HIST + i * 4, 0u);
    else if (i < 72) lds_st<unsigned>(sm, L_MISC + (i - 64) * 4, i == 64 ? 16u : 0u);
    wg_barrier();
  } else if (wave < 15) {
    wg_barrier();
    const int m = (wave - 7) >> 1;
    const us* w = m == 0 ? wb : m == 1 ? wv : m == 2 ? wa : wu;
    const us* p = w + (64 * ((wave - 7) & 1) + 8 * hh) * 128 + fg * 32 + l31;
    unsigned v[32];
#pragma unroll
    for (int j = 0; j < 32; ++j) v[j] = p[(16 * (j >> 3) + (j & 7)) * 128];
#pragma unroll
    for (int s = 0; s < 4; ++s)
      lds_st<u4>(sm, L_WFR + ((wave - 7) * 4 + s) * 1024 + lane * 16,
                 u4{v[8 * s] | (v[8 * s + 1] << 16), v[8 * s + 2] | (v[8 * s + 3] << 16),
                    v[8 * s + 4] | (v[8 * s + 5] << 16), v[8 * s + 6] | (v[8 * s + 7] << 16)});
    lds_signal(sm, M_WDONE, 1u, lane);
    ST(0, 1)
  } else {
    __builtin_amdgcn_s_setprio(3);
    const unsigned xv0 = (lane >> 4) * 256 + (((lane & 15) ^ (lane >> 4)) << 4);
    const unsigned xla = (unsigned)(size_t)sm + L_ROWS;
    wg_barrier();
    dma_x8(x, n0 + 384, xv0, xla + 12 * 32 * kRowB);
#pragma unroll 1
    for (int h = 0; h < 24; ++h) {
      dma_x16(x, n0 + h * 16, xv0, xla + (h >> 1) * 32 * kRowB + (h & 1) * 4096);
      asm volatile("s_waitcnt vmcnt(%0)" :: "i"(4 * XP) : "memory");
      if (h >= XP - 1) lds_st<unsigned>(sm, L_MISC + M_READY * 4, 1 + ((h - XP + 1) >> 1));
    }
    ST(0, 1)
#pragma unroll
    for (int k = XP - 1; k >= 0; --k) {
      asm volatile("s_waitcnt vmcnt(%0)" :: "i"(4 * k) : "memory");
      lds_st<unsigned>(sm, L_MISC + M_READY * 4, 1 + ((24 - k) >> 1));
    }
    __builtin_amdgcn_s_setprio(0);
    ST(0, 2)
  }

  bool slow = false;
  if (wave >= 2 && wave < 15) {
    lds_wait_ge(sm, M_WDONE, 8u);
    lds_wait_ge(sm, M_READY, tile == 12 ? 1u : tile + 2u);
    ST(0, 3)
    const int rr = tile == 12 ? (l31 & 7) : l31;
    u4 xf[8];
#pragma unroll
    for (int s = 0; s < 8; ++s) xf[s] = lds_ld<u4>(sm, stage + rr * 256 + (((2 * s + hh) ^ (rr & 15)) << 4));
    f16v a0, a1;
    gemm2(sm, L_WFR + lane * 16, xf, a0, a1);
    lds_order();
    const bool rv = tile < 12 || l31 < 8;
#pragma unroll
    for (int i = 0; i < 4; ++i) {
      u4 o;
#pragma unroll
      for (int j = 0; j < 4; j += 2) {
        const unsigned bb = pack_h(a0[4 * i + j] * kNL2E, a0[4 * i + j + 1] * kNL2E);
        o[j] = pack_near<0>(f2u(a1[4 * i + j] + 0x1p-100f), bb);
        o[j + 1] = pack_near<1>(f2u(a1[4 * i + j + 1] + 0x1p-100f), bb);
      }
      if (rv) lds_st<u4>(sm, stage + l31 * 128 + (((2 * i + hh) ^ (l31 & 7)) << 4), o);
    }
    lds_order();
    {
      const rsrc_t rtt = mk_rsrc(ws + kOffTab + ((size_t)fg * kRows + n0 + tile * 32) * 128, max(0, nb - tile * 32) * 128);
      const unsigned ro = stage + (lane >> 3) * 128 + (((lane & 7) ^ (lane >> 3)) << 4), js = tile == 12 ? 0 : 1024;
      u4 v[4];
#pragma unroll
      for (int j = 0; j < 4; ++j) v[j] = lds_ld<u4>(sm, ro + j * js);
#pragma unroll
      for (int j = 0; j < 4; ++j) __builtin_amdgcn_raw_buffer_store_b128(v[j], rtt, j * 1024 + lane * 16, 0, kSC1);
    }
    if (bucket == 63 && wave == 2) {
#pragma unroll
      for (int j = 0; j < 3; ++j)
        __builtin_amdgcn_raw_buffer_store_b128(u4{0x7c00u, 0x7c00u, 0x7c00u, 0x7c00u}, rtab, kN * 128 + j * 1024 + lane * 16, 0, kSC1);
    }
    ST(0, 4)
    gemm2(sm, L_WFR + 16384 + lane * 16, xf, a0, a1);
    lds_order();
    float am = 0.f;
#pragma unroll
    for (int i = 0; i < 4; ++i) {
      u4 o;
#pragma unroll
      for (int j = 0; j < 4; j += 2) {
        const float p0 = a0[4 * i + j] * kNL2E, p1 = a0[4 * i + j + 1] * kNL2E;
        am = __builtin_elementwise_maximum(__builtin_elementwise_maximum(am, __builtin_fabsf(p0)), __builtin_fabsf(p1));
        const unsigned uu = pack_bf(a1[4 * i + j], a1[4 * i + j + 1]);
        o[j] = pack_near<0>(f2u(__builtin_amdgcn_exp2f(p0)), uu);
        o[j + 1] = pack_near<1>(f2u(__builtin_amdgcn_exp2f(p1)), uu);
      }
      if (rv) lds_st<u4>(sm, stage + l31 * kRowB + ((2 * i + hh) << 4), o);
    }
    slow = !(am <= 80.f);
    const unsigned pad = kN + (lane & 15);
    if (rv) {
#pragma unroll
      for (int j = 0; j < 5; ++j) lds_st<u4>(sm, stage + l31 * kRowB + 128 + hh * 80 + j * 16, u4{pad, pad, pad, pad});
    }
    ST(0, 5)
  }

  lds_wait_ge(sm, M_EDONE, 4u);
  const u2 g = lds_ld<u2>(sm, L_MISC + M_G0 * 4);
  const unsigned f0 = g.x ^ 0x85EBCA6Bu, f1 = g.y ^ 0xC2B2AE35u;
  if (wave >= 2 && wave < 15) {
    vm_drain();
    if (lane == 0 && lds_add(sm, L_MISC + M_TDONE * 4, 1u) == 12u)
      st_agent(ws + kOffFlag + (fg * 64 + bucket) * 128, u2{f0, f1});
    ST(0, 6)
  }
  unsigned rec[17], ad[17];
  const bool recw = wave < 9 && wave != 3;
  if (recw) {
    lds_wait_ge(sm, M_READY, RDY_G);
    const unsigned k0 = g.x ^ 0x9E3779B9u, k1 = g.y ^ 0x7F4A7C15u;
    const int c = (wave - (wave > 3)) * 32 + hh * 16 + (lane & 15);
    u2 gr;
    unsigned spins = 0;
    while (true) {
      gr = ld_agent(ws + kOffSeg + c * 512 + bucket * 8);
      if (__all(gr.y == ((k0 >> 9) | (k1 << 23)) && ((gr.x ^ (k0 << 23)) >> 23) == 0u)) break;
      if (++spins > kSpinCap) {
        gr.x = 0;
        lds_st<unsigned>(sm, L_MISC + M_ABORT * 4, 1u);
        break;
      }
      __builtin_amdgcn_s_sleep(2);
    }
    after_poll();
    lds_wait_ge(sm, M_READY, RDY_R);
    ST(1, 0)
    slow |= (gr.x >> 22) & 1u;
    const unsigned cn = (gr.x >> 11) & 0x7ffu, st = gr.x & 0x7ffu;
    const unsigned b32 = c * kChunkB + st * 4, b16 = c * kChunkB + kCS * 4 + st * 2;
    bool vl[17];
    unsigned n4[17];
#define REC_LOAD(I)                                                                                      \
    {                                                                                                    \
      vl[I] = (unsigned)l31 < row_bcast<I>(cn);                                                          \
      const unsigned o32 = row_bcast<I>(b32) + l31 * 4, o16 = row_bcast<I>(b16) + l31 * 2;               \
      rec[I] = __builtin_amdgcn_raw_buffer_load_b32(rrec, o32, 0, kSC1);                                 \
      n4[I] = __builtin_amdgcn_raw_buffer_load_b16(rrec, o16, 0, kSC1);                                  \
    }
    REC_LOAD(0) REC_LOAD(1) REC_LOAD(2) REC_LOAD(3) REC_LOAD(4) REC_LOAD(5) REC_LOAD(6) REC_LOAD(7)
    REC_LOAD(8) REC_LOAD(9) REC_LOAD(10) REC_LOAD(11) REC_LOAD(12) REC_LOAD(13) REC_LOAD(14) REC_LOAD(15)
#undef REC_LOAD
    {
      u64 om = __ballot(cn > 32u && !(lane & 16));
      unsigned pos = 0, o32 = kOOB, o16 = kOOB;
      while (om) {
        const int sl = __builtin_ctzll(om);
        om &= om - 1;
        const unsigned ov = __builtin_amdgcn_readlane(cn, sl) - 32u, k = lane - pos;
        if (k < ov) {
          o32 = __builtin_amdgcn_readlane(b32, sl) + (32u + k) * 4;
          o16 = __builtin_amdgcn_readlane(b16, sl) + (32u + k) * 2;
        }
        pos += ov;
      }
      slow |= pos > 64u;
      vl[16] = (unsigned)lane < pos;
      rec[16] = __builtin_amdgcn_raw_buffer_load_b32(rrec, o32, 0, kSC1);
      n4[16] = __builtin_amdgcn_raw_buffer_load_b16(rrec, o16, 0, kSC1);
    }
#pragma unroll
    for (int i = 0; i < 17; ++i) ad[i] = lds_add(sm, L_CNT + (vl[i] ? n4[i] : 1568u + l31 * 4), 4u);
    ST(1, 1)
  }
  if (slow) lds_st<unsigned>(sm, L_MISC + M_SLOW * 4, 1u);
  wg_barrier();
  ST(1, 2)

  if (recw) {
#pragma unroll
    for (int i = 0; i < 17; ++i) lds_st<unsigned>(sm, min(ad[i], (unsigned)L_TRASH), rec[i]);
    if (b == 0 && t == 0) {
      const u64 gn = (((u64)g.y << 32) | g.x) + 1;
      st_agent(ws + kOffEpoch, u2{(unsigned)gn, (unsigned)(gn >> 32)});
    }
  } else {
    if (wave == 15) {
      __builtin_amdgcn_s_setprio(3);
      unsigned dg[7], rk[7];
      bool big = false;
#pragma unroll
      for (int k = 0; k < 7; ++k) {
        const int j = min(lane + 64 * k, 391);
        dg[k] = (lds_ld<unsigned>(sm, L_CNT + j * 4) - (L_ROWS + 128 + j * kRowB)) >> 2;
        big |= dg[k] > (unsigned)kCap;
        dg[k] = min(dg[k], 63u);
      }
#pragma unroll
      for (int k = 0; k < 7; ++k) {
        rk[k] = 0;
        if (lane + 64 * k < ng * 8) rk[k] = lds_add(sm, L_HIST + dg[k] * 4, 1u);
      }
      lds_order();
      const unsigned above = ng * 8 - wave_scan(lds_ld<unsigned>(sm, L_HIST + lane * 4));
#pragma unroll
      for (int k = 0; k < 7; ++k) {
        const unsigned pos = __builtin_amdgcn_ds_bpermute(dg[k] << 2, above) + rk[k];
        if (lane + 64 * k < ng * 8) lds_st<unsigned>(sm, L_ORD + pos * 4, (lane + 64 * k) | (dg[k] << 16));
      }
      if (big) lds_st<unsigned>(sm, L_MISC + M_SLOW * 4, 1u);
    } else if (wave == 10) {
      unsigned spins = 0;
      while (true) {
        const u2 f = ld_agent(ws + kOffFlag + fg * 8192 + lane * 128);
        if (__all(f.x == f0 && f.y == f1)) break;
        if (++spins > kSpinCap) {
          lds_st<unsigned>(sm, L_MISC + M_ABORT * 4, 1u);
          break;
        }
        __builtin_amdgcn_s_sleep(2);
      }
      after_poll();
    }
  }
  const int q = lane & 7;
  const u2 craw = *(const u2*)(wc + fg * 32 + 4 * q);
  ST(1, 3)
  wg_barrier();
  ST(1, 4)
  if (lds_ld<unsigned>(sm, L_MISC + M_ABORT * 4)) return;

  if (lds_ld<unsigned>(sm, L_MISC + M_SLOW * 4) == 0) {
    const float cs = kNL2E * u2f((254u << 23) - lds_ld<unsigned>(sm, L_MISC + M_ESC * 4));
    const f4 cn = {bf_lo(craw.x) * cs, bf_hi(craw.x) * cs, bf_lo(craw.y) * cs, bf_hi(craw.y) * cs};
    const rsrc_t rxs = mk_rsrc(x + (size_t)n0 * 128, nb * 256), ro = mk_rsrc(out + (size_t)n0 * 128, nb * 256);
    const unsigned qoff = q * 16, xoff = fg * 64 + q * 8, oo = L_ORD + (lane >> 3) * 4;
    STC(0)
    int gi = wave;
    __builtin_amdgcn_s_setprio(3);
    while (gi < ng) {
      const unsigned ent = lds_ld<unsigned>(sm, oo + gi * 32);
      const unsigned row = L_ROWS + (ent & 0xffffu) * kRowB, xo = (ent & 0xffffu) * 256 + xoff;
      const int w = __builtin_amdgcn_readfirstlane(ent) >> 16;
      const int nch = (w + 3) >> 2;
      u4 r = lds_ld<u4>(sm, row + 128);
      const u4 au = lds_ld<u4>(sm, row + qoff);
      u4 w0 = gather(rtab, r.x, qoff), w1 = gather(rtab, r.y, qoff), w2 = gather(rtab, r.z, qoff), w3 = gather(rtab, r.w, qoff);
      const u2 xs = __builtin_amdgcn_raw_buffer_load_b64(rxs, xo, 0, XAUX);
      f2 n01 = {0.f, 0.f}, n23 = {0.f, 0.f}, d01 = {1e-6f, 1e-6f}, d23 = {1e-6f, 1e-6f};
      const f2 ea01 = {u2f(au.x), u2f(au.y)}, ea23 = {u2f(au.z), u2f(au.w)};
#pragma unroll 1
      for (int ch = 1; ch < nch; ++ch) {
        const u4 rn = lds_ld<u4>(sm, row + 128 + ch * 16);
        edge_step(w0, r.x, cn, ea01, ea23, n01, n23, d01, d23);
        w0 = gather(rtab, rn.x, qoff);
        __builtin_amdgcn_sched_barrier(0);
        edge_step(w1, r.y, cn, ea01, ea23, n01, n23, d01, d23);
        w1 = gather(rtab, rn.y, qoff);
        __builtin_amdgcn_sched_barrier(0);
        edge_step(w2, r.z, cn, ea01, ea23, n01, n23, d01, d23);
        w2 = gather(rtab, rn.z, qoff);
        __builtin_amdgcn_sched_barrier(0);
        edge_step(w3, r.w, cn, ea01, ea23, n01, n23, d01, d23);
        w3 = gather(rtab, rn.w, qoff);
        __builtin_amdgcn_sched_barrier(0);
        r = rn;
      }
      unsigned gn = 0;
      if (lane == 0) gn = lds_add(sm, L_MISC + M_QUEUE * 4, 1u);
      const int rem = w - 4 * (nch - 1);
      edge_step(w0, r.x, cn, ea01, ea23, n01, n23, d01, d23);
      if (rem > 1) edge_step(w1, r.y, cn, ea01, ea23, n01, n23, d01, d23);
      if (rem > 2) edge_step(w2, r.z, cn, ea01, ea23, n01, n23, d01, d23);
      if (rem > 3) edge_step(w3, r.w, cn, ea01, ea23, n01, n23, d01, d23);
      const f2 z01 = pk_fma(n01, f2{__builtin_amdgcn_rcpf(d01.x), __builtin_amdgcn_rcpf(d01.y)}, f2{bf_lo(au.x), bf_lo(au.y)});
      const f2 z23 = pk_fma(n23, f2{__builtin_amdgcn_rcpf(d23.x), __builtin_amdgcn_rcpf(d23.y)}, f2{bf_lo(au.z), bf_lo(au.w)});
      const f2 o01 = f2{relu(z01.x), relu(z01.y)} + f2{bf_lo(xs.x), bf_hi(xs.x)};
      const f2 o23 = f2{relu(z23.x), relu(z23.y)} + f2{bf_lo(xs.y), bf_hi(xs.y)};
      __builtin_amdgcn_raw_buffer_store_b64(u2{pack_bf(o01.x, o01.y), pack_bf(o23.x, o23.y)}, ro, xo, 0, OAUX);
      gi = __builtin_amdgcn_readfirstlane(gn);
      if (gi < 32) __builtin_amdgcn_s_setprio(2);
      else if (gi < 42) __builtin_amdgcn_s_setprio(1);
      else __builtin_amdgcn_s_setprio(0);
    }
    ST(1, 7)
    STC(1)
    ST_FLUSH
    return;
  }

#pragma unroll 1
  for (int pass = 0; pass < 2; ++pass) {
    const int fb = fg * 32 + pass * 16;
#pragma unroll 1
    for (int i = t; i < nb * 16; i += 1024) {
      const unsigned ra = L_ROWS + (i >> 4) * kRowB + (i & 15) * 4;
      lds_st<float>(sm, ra, kNL2E * dot128(x + (size_t)(n0 + (i >> 4)) * 128, wa + fb + (i & 15)));
      lds_st<float>(sm, ra + 64, 0.f);
      lds_st<float>(sm, ra + 128, 0.f);
    }
    wg_barrier();
#pragma unroll 1
    for (int e = t; e < kE; e += 1024) {
      const unsigned dl = (unsigned)(dst[e] - n0);
      if (dl < (unsigned)nb) {
        const int sn = src[e];
        const float ev = bf_lo(ea[e]);
#pragma unroll 1
        for (int k = 0; k < 16; ++k) {
          const unsigned w = __builtin_amdgcn_raw_buffer_load_b32(rtab, sn * 128 + (pass * 16 + k) * 4, 0, kSC1);
          float bv = h_lo(w);
          if (!(__builtin_fabsf(bv) <= 65504.f)) bv = kNL2E * dot128(x + (size_t)sn * 128, wb + fb + k);
          const unsigned ra = L_ROWS + dl * kRowB + k * 4;
          const float tt = lds_ld<float>(sm, ra) + bv + ev * (kNL2E * bf_lo(wc[fb + k]));
          const float gt = __builtin_amdgcn_rcpf(1.f + __builtin_amdgcn_exp2f(tt));
          __hip_atomic_fetch_add((LAS float*)(sm + ra + 64), gt * u2f(w), __ATOMIC_RELAXED, __HIP_MEMORY_SCOPE_WORKGROUP);
          __hip_atomic_fetch_add((LAS float*)(sm + ra + 128), gt, __ATOMIC_RELAXED, __HIP_MEMORY_SCOPE_WORKGROUP);
        }
      }
    }
    wg_barrier();
#pragma unroll 1
    for (int i = t; i < nb * 16; i += 1024) {
      const unsigned ra = L_ROWS + (i >> 4) * kRowB + (i & 15) * 4;
      const size_t xr = (size_t)(n0 + (i >> 4)) * 128;
      const float z = dot128(x + xr, wu + fb + (i & 15)) + lds_ld<float>(sm, ra + 64) / (lds_ld<float>(sm, ra + 128) + 1e-6f);
      out[xr + fb + (i & 15)] = (us)(pack_bf(bf_lo(x[xr + fb + (i & 15)]) + relu(z), 0.f) & 0xffffu);
    }
    wg_barrier();
  }
}

}

extern "C" void kernel_launch(void* const* d_in, const int* in_sizes, int n_in, void* d_out, int out_size,
                              void* d_ws, size_t ws_size, hipStream_t stream) {
  k_gcn<<<256, 1024, 0, stream>>>((const us*)d_in[0], (const us*)d_in[1], (const int*)d_in[2], (const int*)d_in[3],
                                  (const us*)d_in[4], (const us*)d_in[5], (const us*)d_in[6], (const us*)d_in[7],
                                  (const us*)d_in[8], (char*)d_ws, (us*)d_out);
}
```

```cpp
#include <hip/hip_runtime.h>
#include <hip/hip_bf16.h>
#include <stdint.h>

#define ST(k, i)
#define STC(i)
#define ST_DECL
#define STL(i)
#define ST_ARG
#define ST_PARAM
#define ST_FLUSH

namespace {

typedef float f2 __attribute__((ext_vector_type(2)));
typedef float f4 __attribute__((ext_vector_type(4)));
typedef float f16v __attribute__((ext_vector_type(16)));
typedef unsigned u2 __attribute__((ext_vector_type(2)));
typedef unsigned u3 __attribute__((ext_vector_type(3)));
typedef unsigned u4 __attribute__((ext_vector_type(4)));
typedef __bf16 b8 __attribute__((ext_vector_type(8)));
typedef __bf16 b2 __attribute__((ext_vector_type(2)));
typedef _Float16 h2 __attribute__((ext_vector_type(2)));
typedef short s4 __attribute__((ext_vector_type(4)));
typedef unsigned short us;
typedef unsigned long long u64;
typedef __amdgpu_buffer_rsrc_t rsrc_t;
#define LAS __attribute__((address_space(3)))
#define GAS __attribute__((address_space(1)))
#define DI __device__ __forceinline__

constexpr int kN = 25000, kE = 400000;
constexpr int kBN = 391;
constexpr int kCE = 1564;
constexpr int kStageB = 9776;
constexpr int kChunkB = 9984;
constexpr int kRows = 25024;
constexpr int kCap = 40;
constexpr int kRowB = 288;
constexpr float kNL2E = -1.4426950408889634f;
constexpr unsigned kOOB = 0x80000000u;
constexpr int kSC1 = 16;
constexpr unsigned kSpinCap = 1u << 17;

constexpr size_t kOffTab = 0;
constexpr size_t kOffRec = kOffTab + (size_t)4 * kRows * 128;
constexpr size_t kOffSeg = kOffRec + (size_t)256 * kChunkB;
constexpr size_t kOffFlag = kOffSeg + (size_t)64 * 256 * 8;
constexpr size_t kOffEpoch = kOffFlag + 4 * 64 * 128;

constexpr int L_MISC = 0;
constexpr int L_HIST = 192;
constexpr int L_CNT = L_HIST + 256;
constexpr int L_ORD = L_CNT + 1568 + 128;
constexpr int L_BCNT = L_ORD;
constexpr int L_R32 = L_ORD + 1568;
constexpr int L_W = L_R32 + kStageB;
constexpr int L_XS = L_W;
constexpr int L_ROWS = (L_W + 32768 + 255) & ~255;
constexpr int L_TRASH = L_ROWS + 392 * kRowB;
constexpr int L_END = L_ROWS + 384 * kRowB + 4096;
enum { M_QUEUE = 0, M_SLOW, M_ABORT, M_ECNT, M_EDONE, M_PDONE, M_RDONE, M_SORT, M_WLD, M_WDONE = M_WLD + 2, M_TDONE,
       M_ADONE, M_TX, M_G0 = M_TX + 5, M_G1, M_ESC, M_RDY };
#define RDY_G 7
#define RDY_R 8
#define WDELAY 0
#define WPRIO 3
#define XD 3
#define LSTART 12
#define CODE_LINES 128

DI unsigned f2u(float f) { return __builtin_bit_cast(unsigned, f); }
DI float u2f(unsigned u) { return __builtin_bit_cast(float, u); }
DI float h_lo(unsigned w) { return (float)__builtin_bit_cast(h2, w).x; }
DI float h_hi(unsigned w) { return (float)__builtin_bit_cast(h2, w).y; }
DI unsigned f2h(float f) { return (unsigned)__builtin_bit_cast(us, (_Float16)f); }
DI float bf_lo(unsigned w) { return u2f(w << 16); }
DI float bf_hi(unsigned w) { return u2f(w & 0xffff0000u); }
DI unsigned pack_bf(float lo, float hi) {
  b2 v = {(__bf16)lo, (__bf16)hi};
  return __builtin_bit_cast(unsigned, v);
}
DI f2 pk_fma(f2 a, f2 b, f2 c) { return __builtin_elementwise_fma(a, b, c); }
DI rsrc_t mk_rsrc(const void* p, int bytes) { return __builtin_amdgcn_make_buffer_rsrc((void*)p, (short)0, bytes, 0x00020000); }
DI void wg_barrier() { asm volatile("s_waitcnt lgkmcnt(0)\n\ts_barrier" ::: "memory"); }
DI void vm_drain() { asm volatile("s_waitcnt vmcnt(0)" ::: "memory"); }
DI void lds_order() { asm volatile("" ::: "memory"); }
template <typename T> DI T lds_ld(LAS char* sm, unsigned off) { return *(LAS T*)(sm + off); }
template <typename T> DI void lds_st(LAS char* sm, unsigned off, T v) { *(LAS T*)(sm + off) = v; }
DI unsigned lds_add(LAS char* sm, unsigned off, unsigned v) {
  return __hip_atomic_fetch_add((LAS unsigned*)(sm + off), v, __ATOMIC_RELAXED, __HIP_MEMORY_SCOPE_WORKGROUP);
}
DI unsigned lds_poll(LAS char* sm, int word) { return *(volatile LAS unsigned*)(sm + L_MISC + word * 4); }
DI void lds_wait_ge(LAS char* sm, int word, unsigned v) {
  while ((lds_poll(sm, word) & 0xffu) < v) __builtin_amdgcn_s_sleep(1);
  asm volatile("" ::: "memory");
}
DI void lds_signal(LAS char* sm, int word, unsigned v) {
  u64 keep;
  asm volatile("s_waitcnt lgkmcnt(0)\n\ts_mov_b64 %0, exec\n\ts_mov_b64 exec, 1\n\tds_add_u32 %1, %2\n\ts_mov_b64 exec, %0"
               : "=&s"(keep) : "v"((unsigned)(size_t)sm + L_MISC + word * 4), "v"(v) : "memory");
}
DI unsigned lds_ticket(LAS char* sm, int word) {
  u64 keep;
  unsigned r;
  asm volatile("s_waitcnt lgkmcnt(0)\n\ts_mov_b64 %0, exec\n\ts_mov_b64 exec, 1\n\tds_add_rtn_u32 %1, %2, %3\n\t"
               "s_mov_b64 exec, %0\n\ts_waitcnt lgkmcnt(0)"
               : "=&s"(keep), "=&v"(r) : "v"((unsigned)(size_t)sm + L_MISC + word * 4), "v"(1u) : "memory");
  return __builtin_amdgcn_readfirstlane(r);
}
DI u2 ld_agent(const char* p) {
  const u64 v = __hip_atomic_load((GAS u64*)p, __ATOMIC_RELAXED, __HIP_MEMORY_SCOPE_AGENT);
  return u2{(unsigned)v, (unsigned)(v >> 32)};
}
DI void st_agent(char* p, u2 v) {
  __hip_atomic_store((GAS u64*)p, ((u64)v.y << 32) | v.x, __ATOMIC_RELAXED, __HIP_MEMORY_SCOPE_AGENT);
}
DI void after_poll() {
  __builtin_amdgcn_fence(__ATOMIC_ACQUIRE, "wavefront");
  asm volatile("" ::: "memory");
}
DI unsigned pack_h(float lo, float hi) {
  h2 v = {(_Float16)lo, (_Float16)hi};
  return __builtin_bit_cast(unsigned, v);
}

DI unsigned wave_scan(unsigned v) {
  v += __builtin_amdgcn_update_dpp(0u, v, 0x111, 0xf, 0xf, true);
  v += __builtin_amdgcn_update_dpp(0u, v, 0x112, 0xf, 0xf, true);
  v += __builtin_amdgcn_update_dpp(0u, v, 0x114, 0xf, 0xf, true);
  v += __builtin_amdgcn_update_dpp(0u, v, 0x118, 0xf, 0xf, true);
  v += __builtin_amdgcn_update_dpp(0u, v, 0x142, 0xa, 0xf, false);
  v += __builtin_amdgcn_update_dpp(0u, v, 0x143, 0xc, 0xf, false);
  return v;
}
DI unsigned wave_max(unsigned v) {
  v = max(v, (unsigned)__builtin_amdgcn_update_dpp(0u, v, 0x111, 0xf, 0xf, true));
  v = max(v, (unsigned)__builtin_amdgcn_update_dpp(0u, v, 0x112, 0xf, 0xf, true));
  v = max(v, (unsigned)__builtin_amdgcn_update_dpp(0u, v, 0x114, 0xf, 0xf, true));
  v = max(v, (unsigned)__builtin_amdgcn_update_dpp(0u, v, 0x118, 0xf, 0xf, true));
  v = max(v, (unsigned)__builtin_amdgcn_update_dpp(0u, v, 0x142, 0xa, 0xf, false));
  v = max(v, (unsigned)__builtin_amdgcn_update_dpp(0u, v, 0x143, 0xc, 0xf, false));
  return __builtin_amdgcn_readlane(v, 63);
}
template <int I> DI unsigned row_bcast(unsigned v) { return __builtin_amdgcn_update_dpp(0u, v, 0x150 + I, 0xf, 0xf, true); }

DI void dma4(rsrc_t r, unsigned v0, unsigned v1, unsigned v2, unsigned v3, unsigned ldsa) {
  unsigned keep;
  asm volatile(
      "s_nop 4\n\ts_mov_b32 %0, m0\n\ts_mov_b32 m0, %1\n\ts_nop 0\n\t"
      "buffer_load_dwordx4 %2, %6, 0 offen lds\n\t"
      "buffer_load_dwordx4 %3, %6, 0 offen offset:1024 lds\n\t"
      "buffer_load_dwordx4 %4, %6, 0 offen offset:2048 lds\n\t"
      "buffer_load_dwordx4 %5, %6, 0 offen offset:3072 lds\n\t"
      "s_mov_b32 m0, %0"
      : "=&s"(keep) : "s"(ldsa), "v"(v0), "v"(v1), "v"(v2), "v"(v3), "s"(r) : "memory");
}
DI u2 tr_read(LAS char* sm, unsigned off) {
  return __builtin_bit_cast(u2, __builtin_amdgcn_ds_read_tr16_b64_v4i16((LAS s4*)(sm + off)));
}
DI f16v mfma(u4 a, u4 b, f16v c) {
  return __builtin_amdgcn_mfma_f32_32x32x16_bf16(__builtin_bit_cast(b8, a), __builtin_bit_cast(b8, b), c, 0, 0, 0);
}

DI u4 gather(rsrc_t rt, unsigned rec, unsigned qoff) {
  unsigned o;
  asm("v_mad_u32_u16 %0, %1, %2, %3" : "=v"(o) : "v"(rec), "s"(128u), "v"(qoff));
  return __builtin_amdgcn_raw_buffer_load_b128(rt, o, 0, kSC1);
}

struct Acc { f2 p01, p23, s01, s23, q01, q23; };
DI void gate_den(u4 w, unsigned rec, const f4& c, f2 ea01, f2 ea23, f2& d01, f2& d23) {
  const float e = h_hi(rec);
  float y[4];
#pragma unroll
  for (int j = 0; j < 4; ++j) y[j] = __builtin_fmaf(e, c[j], h_lo(w[j]));
#pragma unroll
  for (int j = 0; j < 4; ++j) y[j] = __builtin_amdgcn_exp2f(y[j]);
  d01 = pk_fma(f2{y[0], y[1]}, ea01, f2{1.f, 1.f});
  d23 = pk_fma(f2{y[2], y[3]}, ea23, f2{1.f, 1.f});
}
DI void edge_step(u4 w, unsigned rec, const f4& c, f2 ea01, f2 ea23, Acc& a) {
  f2 d01, d23;
  gate_den(w, rec, c, ea01, ea23, d01, d23);
  a.q01 = pk_fma(a.q01, d01, f2{u2f(w.x), u2f(w.y)} * a.p01);
  a.q23 = pk_fma(a.q23, d23, f2{u2f(w.z), u2f(w.w)} * a.p23);
  a.s01 = pk_fma(a.s01, d01, a.p01);
  a.s23 = pk_fma(a.s23, d23, a.p23);
  a.p01 *= d01;
  a.p23 *= d23;
}
DI float rescale_f(float p) { return u2f((unsigned)max((int)((164u << 23) - f2u(p)), 0x00800000)); }
DI void rescale(Acc& a) {
  const f2 f01 = {rescale_f(a.p01.x), rescale_f(a.p01.y)}, f23 = {rescale_f(a.p23.x), rescale_f(a.p23.y)};
  a.p01 *= f01; a.s01 *= f01; a.q01 *= f01;
  a.p23 *= f23; a.s23 *= f23; a.q23 *= f23;
}
DI float max3a(float a, float b, float c) {
  return __builtin_elementwise_maximum(__builtin_elementwise_maximum(a, __builtin_fabsf(b)), __builtin_fabsf(c));
}

DI float relu(float z) { return __builtin_elementwise_maximum(z, 0.f); }

DI float dot128(const us* __restrict__ xr, const us* __restrict__ wcol) {
  float acc = 0.f;
#pragma unroll 1
  for (int k = 0; k < 128; ++k) acc = __builtin_fmaf(bf_lo(xr[k]), bf_lo(wcol[k * 128]), acc);
  return acc;
}

template <bool IST> DI bool proj(LAS char* sm, char* ws, int lane, int pj, int fg, int n0, int nb ST_PARAM) {
  u4 wf[16];
  lds_wait_ge(sm, M_WLD + (IST ? 0 : 1), 4u);
  {
    const unsigned wo = L_W + (IST ? 0 : 16384) + (8 * (lane >> 5) + ((lane & 15) >> 2)) * 64 + (lane & 16) * 2 + (lane & 3) * 8;
#pragma unroll
    for (int f = 0; f < 16; ++f) {
      const u2 lo = tr_read(sm, wo + f * 1024), hi = tr_read(sm, wo + f * 1024 + 256);
      wf[f] = u4{lo.x, lo.y, hi.x, hi.y};
    }
  }
  lds_signal(sm, M_WDONE, 1u);
  ST(0, 1)
  bool slow = false;
#pragma unroll 1
  for (int i = 0; i < (pj == 4 ? 1 : 3); ++i) {
    const int seq = 4 * i + pj;
    const int tile = pj == 4 ? 12 : seq + 3 * fg - (seq + 3 * fg >= 12 ? 12 : 0);
    const unsigned stage = L_ROWS + tile * 32 * kRowB;
    int ln = lane;
    asm volatile("" : "+v"(ln));
    const int hh = ln >> 5, rr = ln & (pj == 4 ? 7 : 31);
    const unsigned xa = stage + rr * 256 + ((hh ^ (rr & 15)) << 4);
    lds_wait_ge(sm, M_RDY + (pj == 4 ? 12 : seq), 2u);
    ST(0, 3)
    f16v a0, a1;
#pragma unroll
    for (int k = 0; k < 16; ++k) { a0[k] = 0.f; a1[k] = 0.f; }
    u4 xf[4];
#pragma unroll
    for (int s = 0; s < 4; ++s) xf[s] = lds_ld<u4>(sm, xa ^ (s << 5));
#pragma unroll
    for (int s = 0; s < 8; ++s) {
      a0 = IST ? mfma(xf[s & 3], wf[s], a0) : mfma(wf[s], xf[s & 3], a0);
      a1 = IST ? mfma(xf[s & 3], wf[8 + s], a1) : mfma(wf[8 + s], xf[s & 3], a1);
      __builtin_amdgcn_sched_barrier(0);
      if (s < 4) xf[s] = lds_ld<u4>(sm, xa ^ ((s + 4) << 5));
      if (IST && s == 3) {
        lds_order();
        lds_st<unsigned>(sm, L_MISC + (M_TX + pj) * 4, i + 1);
      }
      if (!IST && s >= 6) xf[s & 1] = lds_ld<u4>(sm, xa ^ (fg << 6) ^ ((s & 1) << 5));
      __builtin_amdgcn_sched_barrier(0);
    }
    if (IST) {
      const rsrc_t rtt = mk_rsrc(ws + kOffTab + ((size_t)fg * kRows + n0 + tile * 32) * 128, max(0, nb - tile * 32) * 128);
      const unsigned so = hh * 512 + (ln & 31) * 4;
#pragma unroll
      for (int k = 0; k < 16; ++k) {
        h2 o = __builtin_bit_cast(h2, f2u(a1[k]));
        o.x = (_Float16)(a0[k] * kNL2E);
        __builtin_amdgcn_raw_buffer_store_b32(__builtin_bit_cast(unsigned, o), rtt, so + ((k & 3) + 8 * (k >> 2)) * 128, 0, kSC1);
      }
      ST(0, 4)
    } else {
      lds_wait_ge(sm, M_TX + pj, i + 1);
      if (i == 0) lds_wait_ge(sm, M_WDONE, 10u);
      const unsigned ra = stage + rr * kRowB + hh * 16, xo = L_XS + (tile * 32 + rr) * 64 + hh * 16;
      lds_st<u4>(sm, xo, xf[0]);
      lds_st<u4>(sm, xo + 32, xf[1]);
      float am = 0.f;
#pragma unroll
      for (int k = 0; k < 4; ++k) {
        u4 o;
#pragma unroll
        for (int j = 0; j < 4; j += 2) {
          const f2 p = pk_fma(f2{a0[4 * k + j], a0[4 * k + j + 1]}, f2{kNL2E, kNL2E}, f2{-0.004f, -0.004f});
          am = max3a(am, p.x, p.y);
          o[j] = pack_bf(a1[4 * k + j], __builtin_amdgcn_exp2f(p.x));
          o[j + 1] = pack_bf(a1[4 * k + j + 1], __builtin_amdgcn_exp2f(p.y));
        }
        lds_st<u4>(sm, ra + k * 32, o);
      }
      slow |= !(am <= 80.f);
      lds_order();
      ST(0, 5)
    }
  }
  return slow;
}

__global__ void __launch_bounds__(1024, 4)
k_gcn(const us* __restrict__ x, const us* __restrict__ ea, const int* __restrict__ src, const int* __restrict__ dst,
      const us* __restrict__ wa, const us* __restrict__ wb, const us* __restrict__ wc, const us* __restrict__ wu,
      const us* __restrict__ wv, char* ws, us* __restrict__ out) {
  __shared__ __attribute__((aligned(16))) char smem[L_END];
  LAS char* sm = (LAS char*)smem;
  const int t = threadIdx.x, lane = t & 63, wave = __builtin_amdgcn_readfirstlane(t >> 6);
  const int b = blockIdx.x, fg = b & 3, bucket = ((b >> 2) & 1) * 32 + (b >> 3), n0 = bucket * kBN;
  const int nb = min(kBN, kN - n0), ng = (nb + 7) >> 3;
  const rsrc_t rtab = mk_rsrc(ws + kOffTab + (size_t)fg * kRows * 128, kN * 128);
  const unsigned la = (unsigned)(size_t)sm;
  u64 pc0;
  asm volatile("s_getpc_b64 %0" : "=s"(pc0));
  bool slow = false;
  ST_DECL
  ST(0, 0)

  const unsigned xv = (lane >> 4) * 256 + (((lane & 15) ^ (lane >> 4)) << 4);
  const rsrc_t rx = mk_rsrc(x + (size_t)n0 * 128, min(392, kN - n0) * 256);
  auto x_piece = [&](int k) {
    int tile = k + 3 * fg;
    tile = k == 12 ? 12 : tile - (tile >= 12 ? 12 : 0);
    const unsigned v = xv + tile * 8192 + (wave & 1) * 4096;
    dma4(rx, v, v ^ 64u, v ^ 128u, v ^ 192u, la + L_ROWS + tile * 32 * kRowB + (wave & 1) * 4096);
  };
  bool ok[2];
  u4 dq[2], sq[2];
  u2 eq[2], g;
  unsigned cw;
  if (wave < 4) {
    __builtin_amdgcn_s_setprio(2);
    const rsrc_t rd = mk_rsrc(dst, kE * 4), rs = mk_rsrc(src, kE * 4), re = mk_rsrc(ea, kE * 2);
#pragma unroll
    for (int r = 0; r < 2; ++r) {
      const int ql = lane + 64 * r, qd = wave * 98 + ql, e0 = b * kCE + 4 * qd;
      ok[r] = ql < 98 && qd < kBN && e0 < kE;
      const unsigned o = ok[r] ? e0 * 2 : kOOB >> 1;
      dq[r] = __builtin_amdgcn_raw_buffer_load_b128(rd, o * 2, 0, 0);
      sq[r] = __builtin_amdgcn_raw_buffer_load_b128(rs, o * 2, 0, 0);
      eq[r] = __builtin_amdgcn_raw_buffer_load_b64(re, o, 0, 0);
    }
    cw = ((const unsigned*)wc)[lane];
    g = ld_agent(ws + kOffEpoch);
    lds_st<unsigned>(sm, L_BCNT + wave * 256 + lane * 4, 0u);
  } else {
    if (wave < 10 || wave == 12) x_piece(wave == 12 ? 12 : (wave - 4) >> 1);
    if (wave == 13) {
      const int id = (b >> 3) * 16 + (lane & 15), m = b >> 6;
      const us* w = m == 0 ? wb : m == 1 ? wv : m == 2 ? wa : wu;
      const u64 pw = (u64)(w + (id & 127) * 128 + fg * 32);
      const u64 pc = (pc0 & ~(u64)127) + min((b >> 3) * 4 + (lane & 3), CODE_LINES - 1) * 128;
      const u64 p = lane < 16 ? pw : pc;
      u64 keep;
      unsigned keepm;
      asm volatile("s_mov_b64 %0, exec\n\ts_mov_b32 %1, m0\n\ts_mov_b64 exec, 0xfffff\n\ts_mov_b32 m0, %3\n\ts_nop 0\n\t"
                   "global_load_lds_dword %2, off\n\ts_mov_b64 exec, %0\n\ts_mov_b32 m0, %1"
                   : "=&s"(keep), "=&s"(keepm) : "v"(p), "s"(la + L_ORD + 1024) : "memory");
    }
    const int i = t - 256, n = i - L_CNT / 4;
    const unsigned v = n < 0 ? (i == 0 ? 16u : 0u) : n < 392 ? L_ROWS + 128 + n * kRowB : L_TRASH;
    if (n < 424) lds_st<unsigned>(sm, i * 4, v);
  }
  wg_barrier();
  ST(1, 5)

  if (wave >= 14) {
    __builtin_amdgcn_s_setprio(3);
    if (LSTART >= 0) lds_wait_ge(sm, M_RDY + LSTART, 1u);
#pragma unroll 1
    for (int k = 3; k < 12; ++k) {
      x_piece(k);
      if (k >= 2 + XD) {
        asm volatile("s_waitcnt vmcnt(%0)" :: "i"(4 * XD - 4) : "memory");
        lds_signal(sm, M_RDY + k - XD + 1, 1u);
        STL(32 + (wave & 1) * 16 + k - XD + 1)
      }
    }
    ST(0, 1)
#pragma unroll
    for (int k = XD - 2; k >= 0; --k) {
      asm volatile("s_waitcnt vmcnt(%0)" :: "i"(4 * k) : "memory");
      lds_signal(sm, M_RDY + 11 - k, 1u);
      STL(32 + (wave & 1) * 16 + 11 - k)
    }
    __builtin_amdgcn_s_setprio(0);
    ST(0, 2)
  } else if (wave >= 4) {
    if (wave < 12) {
      const int k = wave - 4, m = k >> 1;
      const us* w = m == 0 ? wb : m == 1 ? wv : m == 2 ? wa : wu;
      const unsigned v = (lane >> 2) * 256 + (lane & 3) * 16;
      __builtin_amdgcn_s_setprio(WPRIO);
      if (WDELAY && wave >= 8) __builtin_amdgcn_s_sleep(WDELAY);
      dma4(mk_rsrc(w + (k & 1) * 8192 + fg * 32, 16384 - fg * 64), v, v + 3072, v + 6144, v + 9216, la + L_W + k * 4096);
      if (wave < 10) {
        asm volatile("s_waitcnt vmcnt(4)" ::: "memory");
        lds_signal(sm, M_RDY + (k >> 1), 1u);
        STL(32 + (wave & 1) * 16 + (k >> 1))
      }
      vm_drain();
      lds_signal(sm, M_WLD + (k >> 2), 1u);
    } else {
      vm_drain();
      lds_signal(sm, M_RDY + 12, 1u);
    }
    if (wave < 8 || wave == 12) {
      __builtin_amdgcn_s_setprio(1);
      proj<true>(sm, ws, lane, wave == 12 ? 4 : wave - 4, fg, n0, nb ST_ARG);
      vm_drain();
      ST(0, 6)
      if (lds_ticket(sm, M_TDONE) == 4u) {
        lds_wait_ge(sm, M_EDONE, 4u);
        const u2 ge = lds_ld<u2>(sm, L_MISC + M_G0 * 4);
        const unsigned f0 = ge.x ^ 0x85EBCA6Bu, f1 = ge.y ^ 0xC2B2AE35u;
        if (lane == 0) st_agent(ws + kOffFlag + (fg * 64 + bucket) * 128, u2{f0, f1});
        unsigned spins = 0;
        bool seen = false;
        while (true) {
          if (!seen) {
            const u2 f = ld_agent(ws + kOffFlag + fg * 8192 + lane * 128);
            seen = f.x == f0 && f.y == f1;
          }
          if (__all(seen)) break;
          if (++spins > kSpinCap) {
            lds_st<unsigned>(sm, L_MISC + M_ABORT * 4, 1u);
            break;
          }
          __builtin_amdgcn_s_sleep(2);
        }
        after_poll();
        ST(0, 7)
      }
    } else {
      __builtin_amdgcn_s_setprio(0);
      slow = proj<false>(sm, ws, lane, wave == 13 ? 4 : (wave + 2) & 3, fg, n0, nb ST_ARG);
      lds_signal(sm, M_ADONE, 1u);
    }
  } else {
    unsigned bk[8], rk[8];
#pragma unroll
    for (int i = 0; i < 8; ++i) {
      bk[i] = __umul24(dq[i >> 2][i & 3], 21455u) >> 23;
      rk[i] = 0;
    }
#pragma unroll
    for (int r = 0; r < 2; ++r)
      if (ok[r]) {
#pragma unroll
        for (int j = 0; j < 4; ++j) rk[4 * r + j] = lds_add(sm, L_BCNT + wave * 256 + bk[4 * r + j] * 4, 1u);
      }
    lds_signal(sm, M_ECNT, 1u);
    const unsigned cm = wave_max(max(cw & 0x7fff0000u, (cw << 16) & 0x7fff0000u));
    const bool cbad = cm >= (226u << 23);
    const unsigned ce = (cm < (20u << 23) || cbad) ? (127u << 23) : (cm & 0x7f800000u);
    unsigned r32[8], r16[8];
    float em = 0.f;
#pragma unroll
    for (int i = 0; i < 8; i += 2) {
      const unsigned ew = eq[i >> 2][(i >> 1) & 1];
      const f2 e = f2{bf_lo(ew), bf_hi(ew)} * f2{u2f(ce), u2f(ce)};
      em = max3a(em, e.x, e.y);
      const unsigned eh = pack_h(e.x, e.y);
      r32[i] = __builtin_amdgcn_perm(eh, sq[i >> 2][i & 3], 0x05040100u);
      r32[i + 1] = __builtin_amdgcn_perm(eh, sq[i >> 2][(i + 1) & 3], 0x07060100u);
    }
#pragma unroll
    for (int i = 0; i < 8; ++i) {
      r16[i] = (dq[i >> 2][i & 3] - bk[i] * kBN) * 4;
      asm volatile("" : "+v"(r32[i]), "+v"(r16[i]));
    }
    if (t == 0) {
      lds_st<u2>(sm, L_MISC + M_G0 * 4, g);
      lds_st<unsigned>(sm, L_MISC + M_ESC * 4, ce);
    }
    lds_wait_ge(sm, M_ECNT, 4u);
    ST(0, 1)
    const unsigned c0 = lds_ld<unsigned>(sm, L_BCNT + lane * 4), c1 = lds_ld<unsigned>(sm, L_BCNT + 256 + lane * 4);
    const unsigned c2 = lds_ld<unsigned>(sm, L_BCNT + 512 + lane * 4), c3 = lds_ld<unsigned>(sm, L_BCNT + 768 + lane * 4);
    const unsigned tot = c0 + c1 + c2 + c3, tp = (tot + 1) >> 1, inc = wave_scan(tp), ex = inc - tp;
    const unsigned mine = 2 * ex + (wave > 0 ? c0 : 0u) + (wave > 1 ? c1 : 0u) + (wave > 2 ? c2 : 0u);
    unsigned ps[8];
#pragma unroll
    for (int i = 0; i < 8; ++i) ps[i] = __builtin_amdgcn_ds_bpermute(bk[i] << 2, mine) + rk[i];
#pragma unroll
    for (int r = 0; r < 2; ++r)
      if (ok[r]) {
#pragma unroll
        for (int i = 4 * r; i < 4 * r + 4; ++i) {
          const unsigned a = ps[i] * 6 - (ps[i] & 1) * 2;
          lds_st<unsigned>(sm, L_R32 + a, r32[i]);
          lds_st<us>(sm, L_R32 + 8 + a - (ps[i] & 1) * 2, (us)r16[i]);
        }
      }
    lds_signal(sm, M_EDONE, (cbad || __any(!(em <= 8192.f))) ? 0x101u : 1u);
    lds_wait_ge(sm, M_EDONE, 4u);
    ST(0, 3)
    const unsigned dn = lds_poll(sm, M_EDONE);
    const rsrc_t rch = mk_rsrc(ws + kOffRec + (size_t)b * kChunkB, (__builtin_amdgcn_readlane(inc, 63) * 12 + 15) & ~15);
    u4 v[3];
#pragma unroll
    for (int j = 0; j < 3; ++j) v[j] = lds_ld<u4>(sm, L_R32 + j * 4096 + t * 16);
    __builtin_amdgcn_s_setprio(3);
#pragma unroll
    for (int j = 0; j < 3; ++j) __builtin_amdgcn_raw_buffer_store_b128(v[j], rch, j * 4096 + t * 16, 0, kSC1);
    vm_drain();
    if (lds_ticket(sm, M_PDONE) == 3u) {
      const unsigned k0 = g.x ^ 0x9E3779B9u, k1 = g.y ^ 0x7F4A7C15u;
      st_agent(ws + kOffSeg + b * 512 + lane * 8,
               u2{(k0 << 23) | ((dn >> 8) ? 1u << 22 : 0u) | (tot << 11) | ex, (k0 >> 9) | (k1 << 23)});
    }
    __builtin_amdgcn_s_setprio(0);
    ST(0, 2)
  }

  if (wave < 4 || wave >= 12) {
    lds_wait_ge(sm, M_EDONE, 4u);
    lds_wait_ge(sm, M_RDY + RDY_G, 1u);
    const u2 ge = lds_ld<u2>(sm, L_MISC + M_G0 * 4);
    const unsigned k0 = ge.x ^ 0x9E3779B9u, k1 = ge.y ^ 0x7F4A7C15u;
    const int rw = wave & 7, c = rw * 32 + (lane >> 4) * 8 + (lane & 7);
    unsigned gx = 0, spins = 0;
    bool seen = lane & 8;
    while (true) {
      if (!seen) {
        const u2 gr = ld_agent(ws + kOffSeg + c * 512 + bucket * 8);
        gx = gr.x;
        seen = gr.y == ((k0 >> 9) | (k1 << 23)) && ((gr.x ^ (k0 << 23)) >> 23) == 0u;
      }
      if (__all(seen)) break;
      if (++spins > kSpinCap) {
        gx = 0;
        lds_st<unsigned>(sm, L_MISC + M_ABORT * 4, 1u);
        break;
      }
      __builtin_amdgcn_s_sleep(4);
    }
    after_poll();
    lds_wait_ge(sm, M_RDY + RDY_R, 1u);
    ST(1, 0)
    slow |= (gx >> 22) & 1u;
    const rsrc_t rrec = mk_rsrc(ws + kOffRec, 256 * kChunkB);
    const unsigned cn = (gx >> 11) & 0x7ffu, bo = c * kChunkB + (gx & 0x7ffu) * 12;
    unsigned l12 = (lane & 15) * 12, l2 = (lane & 15) * 2;
    asm volatile("" : "+v"(l12), "+v"(l2));
    u3 rc[9];
    int rem[9];
#define REC_LOAD(I)                                                                          \
    {                                                                                        \
      rem[I] = row_bcast<I>(cn) - l2;                                                        \
      const unsigned o = row_bcast<I>(bo) + l12;                                             \
      rc[I] = __builtin_amdgcn_raw_buffer_load_b96(rrec, rem[I] > 0 ? o : kOOB, 0, kSC1);    \
    }
    REC_LOAD(0) REC_LOAD(1) REC_LOAD(2) REC_LOAD(3) REC_LOAD(4) REC_LOAD(5) REC_LOAD(6) REC_LOAD(7)
#undef REC_LOAD
    {
      u64 om = __ballot(cn > 32u);
      unsigned pos = 0, oo = kOOB;
      rem[8] = 0;
      while (om) {
        const int sl = __builtin_ctzll(om);
        om &= om - 1;
        const unsigned cc = __builtin_amdgcn_readlane(cn, sl) - 32u, ov = (cc + 1) >> 1, k = lane - pos;
        const unsigned o = __builtin_amdgcn_readlane(bo, sl) + 192u + k * 12;
        oo = k < ov ? o : oo;
        rem[8] = k < ov ? (int)(cc - 2 * k) : rem[8];
        pos += ov;
      }
      slow |= pos > 64u;
      rc[8] = __builtin_amdgcn_raw_buffer_load_b96(rrec, oo, 0, kSC1);
    }
    unsigned ad[18];
    const unsigned spare = 1568u + (lane & 31) * 4;
#pragma unroll
    for (int i = 0; i < 9; ++i) {
      ad[2 * i] = lds_add(sm, L_CNT + (rem[i] > 0 ? rc[i][2] & 0xffffu : spare), 4u);
      ad[2 * i + 1] = lds_add(sm, L_CNT + (rem[i] > 1 ? rc[i][2] >> 16 : spare), 4u);
    }
    lds_signal(sm, M_RDONE, 1u);
    ST(1, 1)
    lds_wait_ge(sm, M_RDONE, 8u);
    const int j = rw * 49 + min(lane, 48);
    const bool nv = lane < 49 && j < ng * 8;
    unsigned dg = (lds_ld<unsigned>(sm, L_CNT + j * 4) - (L_ROWS + 128 + j * kRowB)) >> 2, rk = 0;
    slow |= dg > (unsigned)kCap;
    dg = min(dg, 63u);
    if (nv) rk = lds_add(sm, L_HIST + dg * 4, 1u);
    lds_signal(sm, M_SORT, 1u);
    if (b == 0 && t == 0) {
      const u64 gn = (((u64)ge.y << 32) | ge.x) + 1;
      st_agent(ws + kOffEpoch, u2{(unsigned)gn, (unsigned)(gn >> 32)});
    }
    lds_wait_ge(sm, M_SORT, 8u);
    const unsigned above = ng * 8 - wave_scan(lds_ld<unsigned>(sm, L_HIST + lane * 4));
    const unsigned pos = __builtin_amdgcn_ds_bpermute(dg << 2, above) + rk;
    if (nv) lds_st<unsigned>(sm, L_ORD + pos * 4, j | (dg << 16));
    ST(1, 6)
    lds_wait_ge(sm, M_ADONE, 5u);
    ST(1, 2)
#pragma unroll
    for (int i = 0; i < 18; ++i) lds_st<unsigned>(sm, min(ad[i], (unsigned)L_TRASH), rc[i >> 1][i & 1]);
  }
  if (slow) lds_st<unsigned>(sm, L_MISC + M_SLOW * 4, 1u);
  const int q = lane & 7;
  const u2 craw = *(const u2*)(wc + fg * 32 + 4 * q);
  ST(1, 3)
  wg_barrier();
  ST(1, 4)
  if (lds_ld<unsigned>(sm, L_MISC + M_ABORT * 4)) return;

  if (lds_ld<unsigned>(sm, L_MISC + M_SLOW * 4) == 0) {
    const float cs = kNL2E * u2f((254u << 23) - lds_ld<unsigned>(sm, L_MISC + M_ESC * 4));
    const f4 cn = {bf_lo(craw.x) * cs, bf_hi(craw.x) * cs, bf_lo(craw.y) * cs, bf_hi(craw.y) * cs};
    const rsrc_t ro = mk_rsrc(out + (size_t)n0 * 128, nb * 256);
    const unsigned qoff = q * 16, xoff = fg * 64 + q * 8, oo = L_ORD + (lane >> 3) * 4;
    STC(0)
    int gi = wave;
    __builtin_amdgcn_s_setprio(3);
    while (gi < ng) {
      const unsigned ent = lds_ld<unsigned>(sm, oo + gi * 32);
      const unsigned node = ent & 0xffffu, row = L_ROWS + node * kRowB;
      const int dg = ent >> 16;
      const int w = __builtin_amdgcn_readfirstlane(ent) >> 16, wmin = __builtin_amdgcn_readlane(ent, 63) >> 16;
      u4 r = lds_ld<u4>(sm, row + 128);
      const u4 au = lds_ld<u4>(sm, row + qoff);
      u4 w0 = gather(rtab, r.x, qoff), w1 = gather(rtab, r.y, qoff), w2 = gather(rtab, r.z, qoff), w3 = gather(rtab, r.w, qoff);
      Acc a = {{0x1p-90f, 0x1p-90f}, {0x1p-90f, 0x1p-90f}, {0.f, 0.f}, {0.f, 0.f}, {0.f, 0.f}, {0.f, 0.f}};
      const f2 ea01 = {u2f(au.x), u2f(au.y)}, ea23 = {u2f(au.z), u2f(au.w)};
      int s = 0, since = 0;
      const int sfull = min(wmin - 3, w - 4);
#pragma unroll 1
      for (; s < sfull; s += 4) {
        const u4 rn = lds_ld<u4>(sm, row + 144 + s * 4);
        edge_step(w0, r.x, cn, ea01, ea23, a);
        w0 = gather(rtab, rn.x, qoff);
        __builtin_amdgcn_sched_barrier(0);
        edge_step(w1, r.y, cn, ea01, ea23, a);
        w1 = gather(rtab, rn.y, qoff);
        __builtin_amdgcn_sched_barrier(0);
        edge_step(w2, r.z, cn, ea01, ea23, a);
        w2 = gather(rtab, rn.z, qoff);
        __builtin_amdgcn_sched_barrier(0);
        edge_step(w3, r.w, cn, ea01, ea23, a);
        w3 = gather(rtab, rn.w, qoff);
        __builtin_amdgcn_sched_barrier(0);
        r = rn;
        if (++since == 3) {
          since = 0;
          if (w - s > 7) rescale(a);
        }
      }
#pragma unroll 1
      for (; s < w - 4; s += 4) {
        const u4 rn = lds_ld<u4>(sm, row + 144 + s * 4);
        if (s < dg) edge_step(w0, r.x, cn, ea01, ea23, a);
        w0 = gather(rtab, rn.x, qoff);
        if (s + 1 < dg) edge_step(w1, r.y, cn, ea01, ea23, a);
        w1 = gather(rtab, rn.y, qoff);
        if (s + 2 < dg) edge_step(w2, r.z, cn, ea01, ea23, a);
        w2 = gather(rtab, rn.z, qoff);
        if (s + 3 < dg) edge_step(w3, r.w, cn, ea01, ea23, a);
        w3 = gather(rtab, rn.w, qoff);
        r = rn;
        if (++since == 3) {
          since = 0;
          if (w - s > 7) rescale(a);
        }
      }
      unsigned gn = 0;
      if (lane == 0) gn = lds_add(sm, L_MISC + M_QUEUE * 4, 1u);
      const u2 xs = lds_ld<u2>(sm, L_XS + node * 64 + q * 8);
      if (s < dg) edge_step(w0, r.x, cn, ea01, ea23, a);
      if (s + 1 < dg) edge_step(w1, r.y, cn, ea01, ea23, a);
      if (s + 2 < dg) edge_step(w2, r.z, cn, ea01, ea23, a);
      if (s + 3 < dg) edge_step(w3, r.w, cn, ea01, ea23, a);
      const float big = max3a(max3a(max3a(max3a(0.f, a.p01.x, a.p01.y), a.p23.x, a.p23.y), a.q01.x, a.q01.y), a.q23.x, a.q23.y);
      if (__any(!(big < 0x1p120f))) {
        a = Acc{{1.f, 1.f}, {1.f, 1.f}, {0.f, 0.f}, {0.f, 0.f}, {0.f, 0.f}, {0.f, 0.f}};
#pragma unroll 1
        for (int k = 0; k < dg; ++k) {
          const unsigned rc = lds_ld<unsigned>(sm, row + 128 + k * 4);
          const u4 v = gather(rtab, rc, qoff);
          f2 d01, d23;
          gate_den(v, rc, cn, ea01, ea23, d01, d23);
          const f2 g01 = {__builtin_amdgcn_rcpf(d01.x), __builtin_amdgcn_rcpf(d01.y)};
          const f2 g23 = {__builtin_amdgcn_rcpf(d23.x), __builtin_amdgcn_rcpf(d23.y)};
          a.q01 = pk_fma(g01, f2{u2f(v.x), u2f(v.y)}, a.q01);
          a.q23 = pk_fma(g23, f2{u2f(v.z), u2f(v.w)}, a.q23);
          a.s01 += g01;
          a.s23 += g23;
        }
      }
      const f2 d01 = pk_fma(a.p01, f2{1e-6f, 1e-6f}, a.s01), d23 = pk_fma(a.p23, f2{1e-6f, 1e-6f}, a.s23);
      const f2 z01 = pk_fma(a.q01, f2{__builtin_amdgcn_rcpf(d01.x), __builtin_amdgcn_rcpf(d01.y)}, f2{bf_lo(au.x), bf_lo(au.y)});
      const f2 z23 = pk_fma(a.q23, f2{__builtin_amdgcn_rcpf(d23.x), __builtin_amdgcn_rcpf(d23.y)}, f2{bf_lo(au.z), bf_lo(au.w)});
      const f2 o01 = f2{relu(z01.x), relu(z01.y)} + f2{bf_lo(xs.x), bf_hi(xs.x)};
      const f2 o23 = f2{relu(z23.x), relu(z23.y)} + f2{bf_lo(xs.y), bf_hi(xs.y)};
      __builtin_amdgcn_raw_buffer_store_b64(u2{pack_bf(o01.x, o01.y), pack_bf(o23.x, o23.y)}, ro, node * 256 + xoff, 0, 0);
      gi = __builtin_amdgcn_readfirstlane(gn);
      if (gi < 32) __builtin_amdgcn_s_setprio(2);
      else if (gi < 42) __builtin_amdgcn_s_setprio(1);
      else __builtin_amdgcn_s_setprio(0);
    }
    ST(1, 7)
    STC(1)
    ST_FLUSH
    return;
  }

#pragma unroll 1
  for (int pass = 0; pass < 2; ++pass) {
    const int fb = fg * 32 + pass * 16;
#pragma unroll 1
    for (int i = t; i < nb * 16; i += 1024) {
      const unsigned ra = L_ROWS + (i >> 4) * kRowB + (i & 15) * 4;
      lds_st<float>(sm, ra, kNL2E * dot128(x + (size_t)(n0 + (i >> 4)) * 128, wa + fb + (i & 15)));
      lds_st<float>(sm, ra + 64, 0.f);
      lds_st<float>(sm, ra + 128, 0.f);
    }
    wg_barrier();
#pragma unroll 1
    for (int e = t; e < kE; e += 1024) {
      const unsigned dl = (unsigned)(dst[e] - n0);
      if (dl < (unsigned)nb) {
        const int sn = src[e];
        const float ev = bf_lo(ea[e]);
#pragma unroll 1
        for (int k = 0; k < 16; ++k) {
          const unsigned w = __builtin_amdgcn_raw_buffer_load_b32(rtab, sn * 128 + (pass * 16 + k) * 4, 0, kSC1);
          float bv = h_lo(w);
          if (!(__builtin_fabsf(bv) <= 65504.f)) bv = kNL2E * dot128(x + (size_t)sn * 128, wb + fb + k);
          const unsigned ra = L_ROWS + dl * kRowB + k * 4;
          const float tt = lds_ld<float>(sm, ra) + bv + ev * (kNL2E * bf_lo(wc[fb + k]));
          const float gt = __builtin_amdgcn_rcpf(1.f + __builtin_amdgcn_exp2f(tt));
          __hip_atomic_fetch_add((LAS float*)(sm + ra + 64), gt * u2f(w), __ATOMIC_RELAXED, __HIP_MEMORY_SCOPE_WORKGROUP);
          __hip_atomic_fetch_add((LAS float*)(sm + ra + 128), gt, __ATOMIC_RELAXED, __HIP_MEMORY_SCOPE_WORKGROUP);
        }
      }
    }
    wg_barrier();
#pragma unroll 1
    for (int i = t; i < nb * 16; i += 1024) {
      const unsigned ra = L_ROWS + (i >> 4) * kRowB + (i & 15) * 4;
      const size_t xr = (size_t)(n0 + (i >> 4)) * 128;
      const float z = dot128(x + xr, wu + fb + (i & 15)) + lds_ld<float>(sm, ra + 64) / (lds_ld<float>(sm, ra + 128) + 1e-6f);
      out[xr + fb + (i & 15)] = (us)(pack_bf(bf_lo(x[xr + fb + (i & 15)]) + relu(z), 0.f) & 0xffffu);
    }
    wg_barrier();
  }
}

}

extern "C" void kernel_launch(void* const* d_in, const int* in_sizes, int n_in, void* d_out, int out_size,
                              void* d_ws, size_t ws_size, hipStream_t stream) {
  k_gcn<<<256, 1024, 0, stream>>>((const us*)d_in[0], (const us*)d_in[1], (const int*)d_in[2], (const int*)d_in[3],
                                  (const us*)d_in[4], (const us*)d_in[5], (const us*)d_in[6], (const us*)d_in[7],
                                  (const us*)d_in[8], (char*)d_ws, (us*)d_out);
}
```

```cpp
#include <hip/hip_runtime.h>
#include <hip/hip_bf16.h>
#include <stdint.h>

#define ST(k, i) {}
#define STC(i) {}
#define ST_DECL
#define STL(i) {}
#define ST_ARG
#define ST_PARAM
#define ST_FLUSH

namespace {

typedef float f2 __attribute__((ext_vector_type(2)));
typedef float f4 __attribute__((ext_vector_type(4)));
typedef float f16v __attribute__((ext_vector_type(16)));
typedef unsigned u2 __attribute__((ext_vector_type(2)));
typedef unsigned u3 __attribute__((ext_vector_type(3)));
typedef unsigned u4 __attribute__((ext_vector_type(4)));
typedef __bf16 b8 __attribute__((ext_vector_type(8)));
typedef __bf16 b2 __attribute__((ext_vector_type(2)));
typedef _Float16 h2 __attribute__((ext_vector_type(2)));
typedef short s4 __attribute__((ext_vector_type(4)));
typedef unsigned short us;
typedef unsigned long long u64;
typedef __amdgpu_buffer_rsrc_t rsrc_t;
#define LAS __attribute__((address_space(3)))
#define GAS __attribute__((address_space(1)))
#define DI __device__ __forceinline__

constexpr int kN = 25000, kE = 400000;
constexpr int kBN = 196;
constexpr int kCE = 1564;
constexpr int kChunkB = 10368;
constexpr int kSegB = 1152;
constexpr int kRows = 25024;
constexpr int kCap = 40;
constexpr int kRowB = 416;
constexpr float kNL2E = -1.4426950408889634f;
constexpr unsigned kOOB = 0x80000000u;
constexpr int kSC1 = 16;
constexpr unsigned kSpinCap = 1u << 17;

constexpr size_t kOffTab = 0;
constexpr size_t kOffRec = kOffTab + (size_t)4 * kRows * 128;
constexpr size_t kOffSeg = kOffRec + (size_t)256 * kChunkB;
constexpr size_t kOffFlag = kOffSeg + (size_t)256 * kSegB;
constexpr size_t kOffEpoch = kOffFlag + 4 * 128 * 128;

constexpr int L_MISC = 0;
constexpr int L_HIST = 192;
constexpr int L_BCNT = L_HIST + 256;
constexpr int L_CNT = L_BCNT + 512;
constexpr int L_ORD = L_CNT + 928;
constexpr int L_JUNK = L_BCNT + 256;
constexpr int L_CF = L_ORD + 1600;
constexpr int L_X = 3840;
constexpr int L_ROWS = L_X + 200 * 256;
constexpr int L_W = L_ROWS;
constexpr int L_R32 = L_ROWS + 65536;
constexpr int L_TRASH = L_ROWS + 200 * kRowB;
constexpr int L_END = L_TRASH + 16;
static_assert(L_CF + 256 + 80 <= L_X && (L_X & 255) == 0 && L_END <= 163840 && L_R32 + 12288 <= L_TRASH, "LDS map");
enum { M_QUEUE = 0, M_ECNT, M_GO, M_GO2, M_EDONE, M_PDONE, M_RDONE, M_SORT, M_WDONE, M_WLD, M_TDONE = M_WLD + 2,
       M_G0 = M_TDONE + 3, M_G1, M_RDY };
static_assert((M_G0 & 1) == 0 && M_RDY + 7 <= 48, "control words");
constexpr unsigned kSlow = 0x100u, kAbort = 0x10000u;
#define NSENT 8
#define PSLEEP 2
#define GDELAY 22
#define FDELAY 28
#define PF1 2
#define PRI1 25
#define PRI0 38
#define T2_GATE 4
#define A2_GATE 3
#define RTAB rtab

DI unsigned f2u(float f) { return __builtin_bit_cast(unsigned, f); }
DI float u2f(unsigned u) { return __builtin_bit_cast(float, u); }
DI float h_lo(unsigned w) { return (float)__builtin_bit_cast(h2, w).x; }
DI float h_hi(unsigned w) { return (float)__builtin_bit_cast(h2, w).y; }
DI unsigned f2h(float f) { return (unsigned)__builtin_bit_cast(us, (_Float16)f); }
DI float bf_lo(unsigned w) { return u2f(w << 16); }
DI float bf_hi(unsigned w) { return u2f(w & 0xffff0000u); }
DI unsigned pack_bf(float lo, float hi) {
  b2 v = {(__bf16)lo, (__bf16)hi};
  return __builtin_bit_cast(unsigned, v);
}
DI f2 pk_fma(f2 a, f2 b, f2 c) { return __builtin_elementwise_fma(a, b, c); }
DI rsrc_t mk_rsrc(const void* p, int bytes) { return __builtin_amdgcn_make_buffer_rsrc((void*)p, (short)0, bytes, 0x00020000); }
DI void wg_barrier() { asm volatile("s_waitcnt lgkmcnt(0)\n\ts_barrier" ::: "memory"); }
DI void vm_drain() { asm volatile("s_waitcnt vmcnt(0)" ::: "memory"); }
DI void lds_order() { asm volatile("" ::: "memory"); }
template <typename T> DI T lds_ld(LAS char* sm, unsigned off) { return *(LAS T*)(sm + off); }
template <typename T> DI void lds_st(LAS char* sm, unsigned off, T v) { *(LAS T*)(sm + off) = v; }
DI unsigned lds_add(LAS char* sm, unsigned off, unsigned v) {
  return __hip_atomic_fetch_add((LAS unsigned*)(sm + off), v, __ATOMIC_RELAXED, __HIP_MEMORY_SCOPE_WORKGROUP);
}
DI unsigned lds_poll(LAS char* sm, int word) { return *(volatile LAS unsigned*)(sm + L_MISC + word * 4); }
DI void lds_wait_ge(LAS char* sm, int word, unsigned v) {
  while ((lds_poll(sm, word) & 0xffu) < v) __builtin_amdgcn_s_sleep(1);
  asm volatile("" ::: "memory");
}
DI void lds_signal(LAS char* sm, int word, unsigned v) {
  u64 keep;
  asm volatile("s_waitcnt lgkmcnt(0)\n\ts_mov_b64 %0, exec\n\ts_mov_b64 exec, 1\n\tds_add_u32 %1, %2\n\ts_mov_b64 exec, %0"
               : "=&s"(keep) : "v"((unsigned)(size_t)sm + L_MISC + word * 4), "v"(v) : "memory");
}
DI unsigned lds_ticket(LAS char* sm, int word) {
  u64 keep;
  unsigned r;
  asm volatile("s_waitcnt lgkmcnt(0)\n\ts_mov_b64 %0, exec\n\ts_mov_b64 exec, 1\n\tds_add_rtn_u32 %1, %2, %3\n\t"
               "s_mov_b64 exec, %0\n\ts_waitcnt lgkmcnt(0)"
               : "=&s"(keep), "=&v"(r) : "v"((unsigned)(size_t)sm + L_MISC + word * 4), "v"(1u) : "memory");
  return __builtin_amdgcn_readfirstlane(r);
}
DI u2 ld_agent(const char* p) {
  const u64 v = __hip_atomic_load((GAS u64*)p, __ATOMIC_RELAXED, __HIP_MEMORY_SCOPE_AGENT);
  return u2{(unsigned)v, (unsigned)(v >> 32)};
}
DI void st_agent(char* p, u2 v) {
  __hip_atomic_store((GAS u64*)p, ((u64)v.y << 32) | v.x, __ATOMIC_RELAXED, __HIP_MEMORY_SCOPE_AGENT);
}
DI void after_poll() {
  __builtin_amdgcn_fence(__ATOMIC_ACQUIRE, "wavefront");
  asm volatile("" ::: "memory");
}
DI unsigned pack_h(float lo, float hi) {
  h2 v = {(_Float16)lo, (_Float16)hi};
  return __builtin_bit_cast(unsigned, v);
}

DI unsigned wave_scan(unsigned v) {
  v += __builtin_amdgcn_update_dpp(0u, v, 0x111, 0xf, 0xf, true);
  v += __builtin_amdgcn_update_dpp(0u, v, 0x112, 0xf, 0xf, true);
  v += __builtin_amdgcn_update_dpp(0u, v, 0x114, 0xf, 0xf, true);
  v += __builtin_amdgcn_update_dpp(0u, v, 0x118, 0xf, 0xf, true);
  v += __builtin_amdgcn_update_dpp(0u, v, 0x142, 0xa, 0xf, false);
  v += __builtin_amdgcn_update_dpp(0u, v, 0x143, 0xc, 0xf, false);
  return v;
}
DI unsigned wave_max(unsigned v) {
  v = max(v, (unsigned)__builtin_amdgcn_update_dpp(0u, v, 0x111, 0xf, 0xf, true));
  v = max(v, (unsigned)__builtin_amdgcn_update_dpp(0u, v, 0x112, 0xf, 0xf, true));
  v = max(v, (unsigned)__builtin_amdgcn_update_dpp(0u, v, 0x114, 0xf, 0xf, true));
  v = max(v, (unsigned)__builtin_amdgcn_update_dpp(0u, v, 0x118, 0xf, 0xf, true));
  v = max(v, (unsigned)__builtin_amdgcn_update_dpp(0u, v, 0x142, 0xa, 0xf, false));
  v = max(v, (unsigned)__builtin_amdgcn_update_dpp(0u, v, 0x143, 0xc, 0xf, false));
  return __builtin_amdgcn_readlane(v, 63);
}
template <int I> DI unsigned oct_bcast(unsigned v) {
  const unsigned lo = __builtin_amdgcn_update_dpp(0u, v, 0x150 + I, 0xf, 0x3, false);
  return __builtin_amdgcn_update_dpp(lo, v, 0x158 + I, 0xf, 0xc, false);
}

DI void dma4(rsrc_t r, unsigned v0, unsigned v1, unsigned v2, unsigned v3, unsigned ldsa) {
  unsigned keep;
  asm volatile(
      "s_nop 4\n\ts_mov_b32 %0, m0\n\ts_mov_b32 m0, %1\n\ts_nop 0\n\t"
      "buffer_load_dwordx4 %2, %6, 0 offen lds\n\t"
      "buffer_load_dwordx4 %3, %6, 0 offen offset:1024 lds\n\t"
      "buffer_load_dwordx4 %4, %6, 0 offen offset:2048 lds\n\t"
      "buffer_load_dwordx4 %5, %6, 0 offen offset:3072 lds\n\t"
      "s_mov_b32 m0, %0"
      : "=&s"(keep) : "s"(ldsa), "v"(v0), "v"(v1), "v"(v2), "v"(v3), "s"(r) : "memory");
}
DI void dma2(rsrc_t r, unsigned v0, unsigned v1, unsigned ldsa) {
  unsigned keep;
  asm volatile(
      "s_nop 4\n\ts_mov_b32 %0, m0\n\ts_mov_b32 m0, %1\n\ts_nop 0\n\t"
      "buffer_load_dwordx4 %2, %4, 0 offen lds\n\t"
      "buffer_load_dwordx4 %3, %4, 0 offen offset:1024 lds\n\t"
      "s_mov_b32 m0, %0"
      : "=&s"(keep) : "s"(ldsa), "v"(v0), "v"(v1), "s"(r) : "memory");
}
DI void touch_lines(rsrc_t r, unsigned v, unsigned ldsa) {
  unsigned keep;
  asm volatile("s_nop 4\n\ts_mov_b32 %0, m0\n\ts_mov_b32 m0, %1\n\ts_nop 0\n\t"
               "buffer_load_dword %2, %3, 0 offen sc1 lds\n\ts_mov_b32 m0, %0"
               : "=&s"(keep) : "s"(ldsa), "v"(v), "s"(r) : "memory");
}
DI unsigned wait_words(const char* p, unsigned tlo, unsigned mlo, unsigned thi, bool& abort) {
  unsigned v = 0, spins = 0;
  bool seen = false, want = true;
  while (true) {
    if (want) {
      const u2 w = ld_agent(p);
      v = w.x;
      seen = w.y == thi && ((w.x ^ tlo) & mlo) == 0u;
    }
    const u64 miss = __ballot(!seen);
    if (miss == 0) break;
    if (__any(want && !seen)) {
      if (++spins > kSpinCap) {
        v = 0;
        abort = true;
        break;
      }
      const u64 wm = __ballot(want && !seen);
      want = want && !seen && __builtin_amdgcn_mbcnt_hi((unsigned)(wm >> 32), __builtin_amdgcn_mbcnt_lo((unsigned)wm, 0u)) < NSENT;
      __builtin_amdgcn_s_sleep(PSLEEP);
    } else {
      want = !seen;
    }
  }
  after_poll();
  return v;
}
DI u2 table_tag(LAS char* sm) {
  const u2 g = lds_ld<u2>(sm, L_MISC + M_G0 * 4);
  return u2{g.x ^ 0x85EBCA6Bu, g.y ^ 0xC2B2AE35u};
}
DI u2 tr_read(LAS char* sm, unsigned off) {
  return __builtin_bit_cast(u2, __builtin_amdgcn_ds_read_tr16_b64_v4i16((LAS s4*)(sm + off)));
}
DI f16v mfma(u4 a, u4 b, f16v c) {
  return __builtin_amdgcn_mfma_f32_32x32x16_bf16(__builtin_bit_cast(b8, a), __builtin_bit_cast(b8, b), c, 0, 0, 0);
}

DI u4 gather(rsrc_t rt, unsigned rec, unsigned qoff) {
  unsigned o;
  asm("v_mad_u32_u16 %0, %1, %2, %3" : "=v"(o) : "v"(rec), "s"(128u), "v"(qoff));
  return __builtin_amdgcn_raw_buffer_load_b128(rt, o, 0, kSC1);
}

DI void gate_den(u4 w, unsigned rec, const f4& c, f2 ea01, f2 ea23, f2& d01, f2& d23) {
  const float e = h_hi(rec);
  float y[4];
#pragma unroll
  for (int j = 0; j < 4; ++j) y[j] = __builtin_fmaf(e, c[j], h_lo(w[j]));
#pragma unroll
  for (int j = 0; j < 4; ++j) y[j] = __builtin_amdgcn_exp2f(y[j]);
  d01 = pk_fma(f2{y[0], y[1]}, ea01, f2{1.f, 1.f});
  d23 = pk_fma(f2{y[2], y[3]}, ea23, f2{1.f, 1.f});
}
DI float max3a(float a, float b, float c) {
  return __builtin_elementwise_maximum(__builtin_elementwise_maximum(a, __builtin_fabsf(b)), __builtin_fabsf(c));
}

DI float relu(float z) { return __builtin_elementwise_maximum(z, 0.f); }

DI float dot128(const us* __restrict__ xr, const us* __restrict__ wcol) {
  float acc = 0.f;
#pragma unroll 1
  for (int k = 0; k < 128; ++k) acc = __builtin_fmaf(bf_lo(xr[k]), bf_lo(wcol[k * 128]), acc);
  return acc;
}

DI void edge_groups(int& gi, int& bad, int ng, rsrc_t rtab, rsrc_t ro, f4 c, unsigned qoff, unsigned ordb, unsigned base,
                    unsigned baseq, unsigned xsk, unsigned xoff, unsigned qaddr) {
  asm volatile(R"ASM(
.set gR, 88
.set gW, 64
.set gD, 6
.set gQ, 60
.set gS, 56
.set gP, 52
.set gA, 48
.set gT, 44
.set gH, 40
.set gTmp, 39
.set gEnt, 38
.set gEntN, 36
.set gRow, 33
.set gXs, 34
.set gTk, 33
.set gOut, 32
.macro gcn_gather sl
  v_mad_u32_u16 v[gTmp], v[gR+(\sl)], s76, %[qoff]
  buffer_load_dwordx4 v[gW+4*((\sl)%%gD):gW+4*((\sl)%%gD)+3], v[gTmp], %[rtab], 0 offen sc1
.endm
.macro gcn_math sl, last
  .set gw, gW+4*((\sl)%%gD)
  v_fma_mix_f32 v[gT], v[gR+(\sl)], %[c0], v[gw] op_sel:[1,0,0] op_sel_hi:[1,0,1]
  v_fma_mix_f32 v[gT+1], v[gR+(\sl)], %[c1], v[gw+1] op_sel:[1,0,0] op_sel_hi:[1,0,1]
  v_fma_mix_f32 v[gT+2], v[gR+(\sl)], %[c2], v[gw+2] op_sel:[1,0,0] op_sel_hi:[1,0,1]
  v_exp_f32 v[gT], v[gT]
  v_fma_mix_f32 v[gT+3], v[gR+(\sl)], %[c3], v[gw+3] op_sel:[1,0,0] op_sel_hi:[1,0,1]
  v_exp_f32 v[gT+1], v[gT+1]
  v_pk_mul_f32 v[gH:gH+1], v[gw:gw+1], v[gP:gP+1]
  v_exp_f32 v[gT+2], v[gT+2]
  v_pk_mul_f32 v[gH+2:gH+3], v[gw+2:gw+3], v[gP+2:gP+3]
  v_exp_f32 v[gT+3], v[gT+3]
  v_pk_fma_f32 v[gT:gT+1], v[gT:gT+1], v[gA:gA+1], 1.0 op_sel_hi:[1,1,0]
  v_pk_fma_f32 v[gT+2:gT+3], v[gT+2:gT+3], v[gA+2:gA+3], 1.0 op_sel_hi:[1,1,0]
  v_pk_fma_f32 v[gQ:gQ+1], v[gQ:gQ+1], v[gT:gT+1], v[gH:gH+1]
  v_pk_fma_f32 v[gQ+2:gQ+3], v[gQ+2:gQ+3], v[gT+2:gT+3], v[gH+2:gH+3]
  v_pk_fma_f32 v[gS:gS+1], v[gS:gS+1], v[gT:gT+1], v[gP:gP+1]
  v_pk_fma_f32 v[gS+2:gS+3], v[gS+2:gS+3], v[gT+2:gT+3], v[gP+2:gP+3]
  .if \last == 0
  v_pk_mul_f32 v[gP:gP+1], v[gP:gP+1], v[gT:gT+1]
  v_pk_mul_f32 v[gP+2:gP+3], v[gP+2:gP+3], v[gT+2:gT+3]
  .endif
.endm
.macro gcn_step sl
Lgs%=_\sl:
  s_mov_b64 exec, -1
  gcn_gather (\sl-gD+1)
  s_bitcmp1_b64 s[72:73], \sl
  s_cbranch_scc1 Lgm%=_\sl
Lgb%=_\sl:
  s_waitcnt vmcnt(gD-1)
  gcn_math \sl, 0
.endm
.macro gcn_tail sl, last
  s_bitcmp1_b64 s[72:73], \sl
  s_cbranch_scc1 Lgm%=_\sl
Lgb%=_\sl:
  s_waitcnt vmcnt(\sl)
  gcn_math \sl, \last
.endm
.macro gcn_stub sl, lim
Lgm%=_\sl:
  .if \lim > 0
  s_cmp_le_u32 s68, \lim
  s_cbranch_scc1 Lgn%=_\sl
  v_sub_u32 v[gT], 0x4d000000, v[gP]
  v_sub_u32 v[gT+1], 0x4d000000, v[gP+1]
  v_sub_u32 v[gT+2], 0x4d000000, v[gP+2]
  v_sub_u32 v[gT+3], 0x4d000000, v[gP+3]
  v_max_i32 v[gT], 0x800000, v[gT]
  v_max_i32 v[gT+1], 0x800000, v[gT+1]
  v_max_i32 v[gT+2], 0x800000, v[gT+2]
  v_max_i32 v[gT+3], 0x800000, v[gT+3]
  v_pk_mul_f32 v[gP:gP+1], v[gP:gP+1], v[gT:gT+1]
  v_pk_mul_f32 v[gP+2:gP+3], v[gP+2:gP+3], v[gT+2:gT+3]
  v_pk_mul_f32 v[gS:gS+1], v[gS:gS+1], v[gT:gT+1]
  v_pk_mul_f32 v[gS+2:gS+3], v[gS+2:gS+3], v[gT+2:gT+3]
  v_pk_mul_f32 v[gQ:gQ+1], v[gQ:gQ+1], v[gT:gT+1]
  v_pk_mul_f32 v[gQ+2:gQ+3], v[gQ+2:gQ+3], v[gT+2:gT+3]
  s_cmp_gt_u32 s69, \sl
  s_cbranch_scc1 Lgb%=_\sl
Lgn%=_\sl:
  .endif
  v_cmpx_lt_u32 vcc, ((\sl)<<16)|0xffff, v[gEnt]
  s_branch Lgb%=_\sl
.endm
.macro gcn_first sl
  gcn_gather \sl
  s_cmp_eq_u32 s84, \sl
  s_cbranch_scc1 Lgd%=
.endm

  s_getpc_b64 s[60:61]
Lpc%=:
  s_add_u32 s62, s60, Lge%=-Lpc%=
  s_addc_u32 s63, s61, 0
  s_add_u32 s64, s60, Lre%=-Lpc%=
  s_addc_u32 s65, s61, 0
  s_add_u32 s60, s60, Lse%=-Lpc%=
  s_addc_u32 s61, s61, 0
  s_movk_i32 s76, 128
  s_movk_i32 s77, 416
  s_movk_i32 s78, 256
  s_mov_b32 s80, 0x0d800000
  s_mov_b32 s81, 0x0d800000
  s_mov_b32 s82, 0x038637bd
  s_mov_b32 s83, 0x038637bd
  s_mov_b32 s75, 0
  s_mov_b32 s74, %[g]
  s_mov_b32 %[bad], -1
  s_lshl_b32 s71, s74, 6
  v_add_u32 v[gTmp], s71, %[ordb]
  ds_read_b64 v[gEntN:gEntN+1], v[gTmp]
Lnx%=:
  s_waitcnt lgkmcnt(0)
  v_mad_u32_u16 v[gRow], v[gEntN], s77, %[base]
  v_readfirstlane_b32 s68, v[gEntN]
  v_readlane_b32 s69, v[gEntN], 63
  s_nop 1
  s_lshr_b32 s68, s68, 16
  s_lshr_b32 s69, s69, 16
  s_max_u32 s70, s68, gD
  s_min_u32 s70, s70, 40
  s_add_u32 s71, s70, 3
  s_lshr_b32 s71, s71, 2
  s_lshl_b32 s71, s71, 3
  s_sub_u32 s66, s64, s71
  s_subb_u32 s67, s65, 0
  s_setpc_b64 s[66:67]
.irp i,9,8,7,6,5,4,3,2,1,0
  ds_read_b128 v[gR+4*\i:gR+4*\i+3], v[gRow] offset:256+16*\i
.endr
Lre%=:
  s_cmp_eq_u32 s75, 0
  s_cbranch_scc1 Lst%=
Lep%=:
  v_rcp_f32 v[gT], v[gS]
  v_lshlrev_b32 v[gH], 16, v[gA]
  v_rcp_f32 v[gT+1], v[gS+1]
  v_lshlrev_b32 v[gH+1], 16, v[gA+1]
  v_rcp_f32 v[gT+2], v[gS+2]
  v_lshlrev_b32 v[gH+2], 16, v[gA+2]
  v_rcp_f32 v[gT+3], v[gS+3]
  v_lshlrev_b32 v[gH+3], 16, v[gA+3]
  v_pk_fma_f32 v[gQ:gQ+1], v[gQ:gQ+1], v[gT:gT+1], v[gH:gH+1]
  v_pk_fma_f32 v[gQ+2:gQ+3], v[gQ+2:gQ+3], v[gT+2:gT+3], v[gH+2:gH+3]
  v_lshlrev_b32 v[gH], 16, v[gXs]
  v_and_b32 v[gH+1], 0xffff0000, v[gXs]
  v_lshlrev_b32 v[gH+2], 16, v[gXs+1]
  v_and_b32 v[gH+3], 0xffff0000, v[gXs+1]
  v_maximum3_f32 v[gQ], v[gQ], 0, 0
  v_maximum3_f32 v[gQ+1], v[gQ+1], 0, 0
  v_maximum3_f32 v[gQ+2], v[gQ+2], 0, 0
  v_maximum3_f32 v[gQ+3], v[gQ+3], 0, 0
  v_pk_add_f32 v[gQ:gQ+1], v[gQ:gQ+1], v[gH:gH+1]
  v_pk_add_f32 v[gQ+2:gQ+3], v[gQ+2:gQ+3], v[gH+2:gH+3]
  s_cmp_ge_u32 s74, %[ng]
  v_cvt_pk_bf16_f32 v[gT], v[gQ], v[gQ+1]
  v_cvt_pk_bf16_f32 v[gT+1], v[gQ+2], v[gQ+3]
  buffer_store_dwordx2 v[gT:gT+1], v[gOut], %[ro], 0 offen
  s_cbranch_scc1 Lend%=
Lst%=:
  v_mad_u32_u16 v[gTmp], v[gEntN], s77, %[baseq]
  v_xor_b32 v[gTk], v[gEntN+1], %[xsk]
  s_mov_b32 s75, 1
  s_mov_b32 %[g], s74
  ds_read_b128 v[gA:gA+3], v[gTmp]
  ds_read_b64 v[gXs:gXs+1], v[gTk]
  v_mad_u32_u16 v[gOut], v[gEntN], s78, %[xoff]
  v_mov_b32 v[gEnt], v[gEntN]
  v_mov_b64 v[gP:gP+1], s[80:81]
  v_mov_b64 v[gP+2:gP+3], s[80:81]
  v_mov_b64 v[gS:gS+1], s[82:83]
  v_mov_b64 v[gS+2:gS+3], s[82:83]
  v_mov_b64 v[gQ:gQ+1], 0
  v_mov_b64 v[gQ+2:gQ+3], 0
  s_lshl_b64 s[72:73], -1, s69
  s_cmp_gt_u32 s68, 17
  s_cselect_b32 s71, 0x800, 0
  s_or_b32 s72, s72, s71
  s_cmp_gt_u32 s68, 29
  s_cselect_b32 s71, 0x800000, 0
  s_or_b32 s72, s72, s71
  s_setprio 2
  s_cmp_lt_u32 s74, %[p1]
  s_cbranch_scc1 Lpr%=
  s_setprio 1
  s_cmp_lt_u32 s74, %[p0]
  s_cbranch_scc1 Lpr%=
  s_setprio 0
Lpr%=:
  s_sub_u32 s84, s70, gD-1
  s_mul_i32 s71, s84, Lse%=-Lgs%=_5
  s_sub_u32 s86, s60, s71
  s_subb_u32 s87, s61, 0
  s_sub_u32 s71, s70, 1
  s_mul_i32 s71, s71, 24
  s_sub_u32 s66, s62, s71
  s_subb_u32 s67, s63, 0
  s_waitcnt lgkmcnt(2)
  s_setpc_b64 s[66:67]
.irp i,39,38,37,36,35,34,33,32,31,30,29,28,27,26,25,24,23,22,21,20,19,18,17,16,15,14,13,12,11,10,9,8,7,6,5,4,3,2,1
  gcn_first \i
.endr
Lge%=:
Lgd%=:
  s_waitcnt lgkmcnt(0)
  s_setpc_b64 s[86:87]
.irp i,39,38,37,36,35,34,33,32,31,30,29,28,27,26,25,24,22,21,20,19,18,17,16,15,14,13,12,10,9,8,7,6,5,4,3,2,1,0
  gcn_stub \i, 0
.endr
  gcn_stub 23, 29
  gcn_stub 11, 17
.irp i,39,38,37,36,35,34,33,32,31,30,29,28,27,26,25,24,23,22,21,20,19,18,17,16,15,14,13,12,11,10,9,8,7,6,5
  gcn_step \i
.endr
Lse%=:
  s_mov_b64 exec, 1
  ds_add_rtn_u32 v[gTk], %[qaddr], %[one]
  s_mov_b64 exec, -1
  gcn_tail 4, 0
  s_mov_b64 exec, -1
  gcn_tail 3, 0
  s_mov_b64 exec, -1
  s_waitcnt lgkmcnt(0)
  v_readfirstlane_b32 s74, v[gTk]
  s_nop 1
  s_lshl_b32 s71, s74, 6
  v_add_u32 v[gTmp], s71, %[ordb]
  ds_read_b64 v[gEntN:gEntN+1], v[gTmp]
  gcn_tail 2, 0
  s_mov_b64 exec, -1
  gcn_tail 1, 0
  s_mov_b64 exec, -1
  gcn_tail 0, 1
  s_mov_b64 exec, -1
  v_maximum3_f32 v[gT], |v[gQ]|, |v[gQ+1]|, |v[gQ+2]|
  v_maximum3_f32 v[gT+1], |v[gQ+3]|, v[gS], v[gS+1]
  v_maximum3_f32 v[gT], v[gT], v[gS+2], v[gS+3]
  s_nop 0
  v_maximum3_f32 v[gT], v[gT], v[gT+1], v[gT+1]
  s_nop 0
  v_cmp_ngt_f32 vcc, 0x7e800000, v[gT]
  s_nop 1
  s_cmp_lg_u64 vcc, 0
  s_cbranch_scc1 Lbad%=
  s_cmp_lt_u32 s74, %[ng]
  s_cbranch_scc1 Lnx%=
  s_branch Lep%=
Lbad%=:
  s_mov_b32 %[bad], %[g]
Lend%=:
  s_mov_b32 %[g], s74
  s_waitcnt lgkmcnt(0)
.purgem gcn_gather
.purgem gcn_math
.purgem gcn_step
.purgem gcn_tail
.purgem gcn_stub
.purgem gcn_first
)ASM"
      : [g] "+s"(gi), [bad] "=&s"(bad)
      : [ng] "s"(ng), [rtab] "s"(rtab), [ro] "s"(ro), [c0] "v"(c[0]), [c1] "v"(c[1]), [c2] "v"(c[2]), [c3] "v"(c[3]),
        [qoff] "v"(qoff), [ordb] "v"(ordb), [base] "v"(base), [baseq] "v"(baseq), [xsk] "v"(xsk), [xoff] "v"(xoff),
        [qaddr] "v"(qaddr), [one] "v"(1u), [p1] "n"(PRI1), [p0] "n"(PRI0)
      : "memory", "vcc", "scc",
        "s60", "s61", "s62", "s63", "s64", "s65", "s66", "s67", "s68", "s69", "s70", "s71", "s72", "s73", "s74", "s75", "s76", "s77", "s78", "s79", "s80", "s81", "s82", "s83", "s84", "s85", "s86", "s87",
        "v32", "v33", "v34", "v35", "v36", "v37", "v38", "v39", "v40", "v41", "v42", "v43", "v44", "v45", "v46", "v47",
        "v48", "v49", "v50", "v51", "v52", "v53", "v54", "v55", "v56", "v57", "v58", "v59", "v60", "v61", "v62", "v63",
        "v64", "v65", "v66", "v67", "v68", "v69", "v70", "v71", "v72", "v73", "v74", "v75", "v76", "v77", "v78", "v79",
        "v80", "v81", "v82", "v83", "v84", "v85", "v86", "v87", "v88", "v89", "v90", "v91", "v92", "v93", "v94", "v95",
        "v96", "v97", "v98", "v99", "v100", "v101", "v102", "v103", "v104", "v105", "v106", "v107", "v108", "v109", "v110", "v111",
        "v112", "v113", "v114", "v115", "v116", "v117", "v118", "v119", "v120", "v121", "v122", "v123", "v124", "v125", "v126", "v127");
}

template <bool IST> DI bool proj(LAS char* sm, char* ws, int lane, unsigned tasks, int h, int gs, int n0, int nb ST_PARAM) {
  u4 wf[16];
  lds_wait_ge(sm, M_WLD + (IST ? 0 : 1), 8u);
  {
    const unsigned wo = L_W + (IST ? 0 : 32768) + (8 * (lane >> 5) + ((lane & 15) >> 2)) * 128 + ((h ^ ((lane >> 3) & 1)) << 6) +
                        (lane & 16) * 2 + (lane & 3) * 8;
#pragma unroll
    for (int f = 0; f < 16; ++f) {
      const u2 lo = tr_read(sm, wo + f * 2048), hi = tr_read(sm, wo + f * 2048 + 512);
      wf[f] = u4{lo.x, lo.y, hi.x, hi.y};
    }
  }
  lds_signal(sm, M_WDONE, 1u);
  ST(0, 1)
  if (h) {
    constexpr int gate = IST ? T2_GATE : A2_GATE;
    if (gate == 1) lds_wait_ge(sm, M_TDONE, 1u);
    if (gate == 2) lds_wait_ge(sm, M_RDONE, 1u);
    if (gate == 3) lds_wait_ge(sm, M_GO, 9u);
    if (gate == 4) lds_wait_ge(sm, M_TDONE, 4u);
  }
  bool slow = false, first = true;
#pragma unroll 1
  for (; tasks != 15u; tasks >>= 4) {
    const int seq = tasks & 15u;
    const int tile = seq == 6 ? 6 : seq + 3 * gs - (seq + 3 * gs >= 6 ? 6 : 0);
    int ln = lane;
    asm volatile("" : "+v"(ln));
    const int hh = ln >> 5, rr = ln & (seq == 6 ? 7 : 31);
    const unsigned xa = L_X + tile * 8192 + rr * 256 + ((hh ^ (rr & 15)) << 4);
    lds_wait_ge(sm, M_RDY + seq, 2u);
    ST(0, 3)
    f16v a0, a1;
#pragma unroll
    for (int k = 0; k < 16; ++k) { a0[k] = 0.f; a1[k] = 0.f; }
    u4 xf[4];
#pragma unroll
    for (int s = 0; s < 4; ++s) xf[s] = lds_ld<u4>(sm, xa ^ (s << 5));
#pragma unroll
    for (int s = 0; s < 8; ++s) {
      a0 = IST ? mfma(xf[s & 3], wf[s], a0) : mfma(wf[s], xf[s & 3], a0);
      a1 = IST ? mfma(xf[s & 3], wf[8 + s], a1) : mfma(wf[8 + s], xf[s & 3], a1);
      __builtin_amdgcn_sched_barrier(0);
      if (s < 4) xf[s] = lds_ld<u4>(sm, xa ^ ((s + 4) << 5));
      __builtin_amdgcn_sched_barrier(0);
    }
    if (IST) {
      const rsrc_t rtt = mk_rsrc(ws + kOffTab + ((size_t)(gs * 2 + h) * kRows + n0 + tile * 32) * 128, max(0, nb - tile * 32) * 128);
      const unsigned so = hh * 512 + (ln & 31) * 4;
      asm volatile("s_nop 7\n\ts_nop 3" : "+v"(a0), "+v"(a1));
#pragma unroll
      for (int k = 0; k < 16; ++k) {
        float o = a1[k];
        asm("v_fma_mixlo_f16 %0, %1, %2, 0" : "+v"(o) : "v"(a0[k]), "s"(kNL2E));
        a1[k] = o;
      }
      asm volatile("s_nop 0" : "+v"(a1));
#pragma unroll
      for (int k = 0; k < 16; ++k) __builtin_amdgcn_raw_buffer_store_b32(f2u(a1[k]), rtt, so + ((k & 3) + 8 * (k >> 2)) * 128, 0, kSC1);
      ST(0, 4)
    } else {
      if (first) lds_wait_ge(sm, M_WDONE, 16u);
      first = false;
      const unsigned ra = L_ROWS + (tile * 32 + rr) * kRowB + h * 128 + hh * 16;
      float am = 0.f;
#pragma unroll
      for (int k = 0; k < 4; ++k) {
        u4 o;
#pragma unroll
        for (int m = 0; m < 4; m += 2) {
          const f2 p = pk_fma(f2{a0[4 * k + m], a0[4 * k + m + 1]}, f2{kNL2E, kNL2E}, f2{-0.004f, -0.004f});
          am = max3a(am, p.x, p.y);
          o[m] = pack_bf(a1[4 * k + m], __builtin_amdgcn_exp2f(p.x));
          o[m + 1] = pack_bf(a1[4 * k + m + 1], __builtin_amdgcn_exp2f(p.y));
        }
        lds_st<u4>(sm, ra + k * 32, o);
      }
      slow |= !(am <= 80.f);
      lds_order();
      ST(0, 5)
    }
  }
  return slow;
}

__global__ void __launch_bounds__(1024, 4)
k_gcn(const us* __restrict__ x, const us* __restrict__ ea, const int* __restrict__ src, const int* __restrict__ dst,
      const us* __restrict__ wa, const us* __restrict__ wb, const us* __restrict__ wc, const us* __restrict__ wu,
      const us* __restrict__ wv, char* ws, us* __restrict__ out) {
  __shared__ __attribute__((aligned(256))) char smem[L_END];
  LAS char* sm = (LAS char*)smem;
  const int t = threadIdx.x, lane = t & 63, wave = __builtin_amdgcn_readfirstlane(t >> 6);
  const int b = blockIdx.x, gs = b & 1, bucket = ((b >> 1) & 3) * 32 + (b >> 3), n0 = bucket * kBN;
  const int nb = min(kBN, kN - n0), ng = (nb + 7) >> 3;
  const unsigned la = (unsigned)(size_t)sm;
  u64 pc0;
  asm volatile("s_getpc_b64 %0" : "=s"(pc0));
  bool slow = false, abort = false;
  ST_DECL
  ST(0, 0)

  const unsigned xv = (lane >> 4) * 256 + (((lane & 15) ^ (lane >> 4)) << 4);
  const rsrc_t rx = mk_rsrc(x + (size_t)n0 * 128, min(200, kN - n0) * 256);
  auto x_piece = [&](int p) {
    p += 6 * gs;
    p -= p >= 12 ? 12 : 0;
    const unsigned v = xv + p * 4096;
    dma4(rx, v, v ^ 64u, v ^ 128u, v ^ 192u, la + L_X + p * 4096);
  };
  auto w_piece = [&](int a) {
    const int k = wave - 8;
    const unsigned v = (lane >> 3) * 256 + ((((lane >> 2) ^ (lane >> 4)) & 1) << 6) + (lane & 3) * 16;
    const us* w = a ? (k < 4 ? wa : wu) : (k < 4 ? wb : wv);
    dma4(mk_rsrc(w + (k & 3) * 4096 + gs * 64, 8192 - gs * 128), v, v + 1024, v + 2048, v + 3072, la + L_W + a * 32768 + k * 4096);
  };
  bool ok;
  u4 dq, sq;
  u2 eq, g;
  unsigned cw;
  if (wave < 7) {
    __builtin_amdgcn_s_setprio(3);
    const int e0 = b * kCE + 4 * t;
    ok = t < kCE / 4 && e0 < kE;
    const unsigned o = ok ? e0 * 2 : kOOB >> 1;
    dq = __builtin_amdgcn_raw_buffer_load_b128(mk_rsrc(dst, kE * 4), o * 2, 0, 0);
    sq = __builtin_amdgcn_raw_buffer_load_b128(mk_rsrc(src, kE * 4), o * 2, 0, 0);
    eq = __builtin_amdgcn_raw_buffer_load_b64(mk_rsrc(ea, kE * 2), o, 0, 0);
    cw = ((const unsigned*)wc)[lane];
    g = ld_agent(ws + kOffEpoch);
  } else {
    const int i = t - 448, n = i - L_CNT / 4;
    const unsigned v = n < 0 ? (i == 0 ? 11u : 0u) : n < 200 ? L_ROWS + 256 + n * kRowB : L_TRASH;
    if (n < 232) lds_st<unsigned>(sm, i * 4, v);
  }
  wg_barrier();
  ST(1, 5)

  unsigned c0, c1, ex0, ex1, inc;
  if (wave >= 8) {
    const int k = wave - 8;
    __builtin_amdgcn_s_setprio(3);
    w_piece(0);
    __builtin_amdgcn_s_setprio(2);
    if (k < 6) {
      x_piece(k);
      asm volatile("s_waitcnt vmcnt(4)" ::: "memory");
    } else if (k == 6) {
      dma2(rx, xv + 6 * 8192, (xv ^ 64u) + 6 * 8192, la + L_X + 6 * 8192);
      asm volatile("s_waitcnt vmcnt(2)" ::: "memory");
    } else {
      const u64 p = (pc0 & ~(u64)127) + ((b >> 3) * 5 + min(lane, 4)) * 128;
      u64 keep;
      unsigned keepm;
      asm volatile("s_mov_b64 %0, exec\n\ts_mov_b32 %1, m0\n\ts_mov_b64 exec, 0x1f\n\ts_mov_b32 m0, %3\n\ts_nop 0\n\t"
                   "global_load_lds_dword %2, off\n\ts_mov_b64 exec, %0\n\ts_mov_b32 m0, %1\n\ts_waitcnt vmcnt(1)"
                   : "=&s"(keep), "=&s"(keepm) : "v"(p), "s"(la + L_CF + 256) : "memory");
    }
    lds_signal(sm, M_WLD, 1u);
    __builtin_amdgcn_s_setprio(1);
    w_piece(1);
    asm volatile("s_waitcnt vmcnt(4)" ::: "memory");
    lds_signal(sm, M_RDY + (k < 6 ? k >> 1 : 6), 1u);
    STL(32 + k)
    __builtin_amdgcn_s_setprio(0);
    if (k < 6) {
      x_piece(6 + k);
      asm volatile("s_waitcnt vmcnt(4)" ::: "memory");
      lds_signal(sm, M_WLD + 1, 1u);
      vm_drain();
      lds_signal(sm, M_RDY + 3 + (k >> 1), 1u);
      STL(40 + (k >> 1))
    } else {
      vm_drain();
      lds_signal(sm, M_WLD + 1, 1u);
    }
  } else if (wave < 7) {
    unsigned bk[4], rk[4] = {0u, 0u, 0u, 0u};
#pragma unroll
    for (int i = 0; i < 4; ++i) bk[i] = __umul24(dq[i], 21400u) >> 22;
    if (ok) {
#pragma unroll
      for (int i = 0; i < 4; ++i) rk[i] = lds_add(sm, L_BCNT + bk[i] * 4, 1u);
    }
    lds_signal(sm, M_ECNT, 1u);
    const unsigned cm = wave_max(max(cw & 0x7fff0000u, (cw << 16) & 0x7fff0000u));
    const bool cbad = cm >= (226u << 23);
    const unsigned ce = (cm < (20u << 23) || cbad) ? (127u << 23) : (cm & 0x7f800000u);
    unsigned r32[4], r16[4];
    float em = 0.f;
#pragma unroll
    for (int i = 0; i < 4; i += 2) {
      const unsigned ew = eq[i >> 1];
      const f2 e = f2{bf_lo(ew), bf_hi(ew)} * f2{u2f(ce), u2f(ce)};
      em = max3a(em, e.x, e.y);
      const unsigned eh = pack_h(e.x, e.y);
      r32[i] = __builtin_amdgcn_perm(eh, sq[i], 0x05040100u);
      r32[i + 1] = __builtin_amdgcn_perm(eh, sq[i + 1], 0x07060100u);
    }
#pragma unroll
    for (int i = 0; i < 4; ++i) {
      r16[i] = (dq[i] - bk[i] * kBN) * 4;
      asm volatile("" : "+v"(r32[i]), "+v"(r16[i]));
    }
    if (wave == 6) {
      const float cs = kNL2E * u2f((254u << 23) - ce);
      if ((lane >> 5) == gs) lds_st<f2>(sm, L_CF + (lane & 31) * 8, f2{bf_lo(cw) * cs, bf_hi(cw) * cs});
      lds_st<u2>(sm, L_MISC + M_G0 * 4, g);
    }
    lds_wait_ge(sm, M_ECNT, 7u);
    ST(0, 1)
    const u2 cc = lds_ld<u2>(sm, L_BCNT + lane * 8);
    c0 = cc.x;
    c1 = cc.y;
    const unsigned p0 = (c0 + 1) >> 1, p1 = (c1 + 1) >> 1;
    inc = wave_scan(p0 + p1);
    ex1 = inc - p1;
    ex0 = ex1 - p0;
    const unsigned pk = (ex0 * 2) | (ex1 << 17);
    unsigned ps[4];
#pragma unroll
    for (int i = 0; i < 4; ++i)
      ps[i] = __builtin_amdgcn_ubfe(__builtin_amdgcn_ds_bpermute(bk[i] << 1, pk), (bk[i] & 1) << 4, 16u) + rk[i];
    if (ok) {
#pragma unroll
      for (int i = 0; i < 4; ++i) {
        const unsigned a = ps[i] * 6 - (ps[i] & 1) * 2;
        lds_st<unsigned>(sm, L_R32 + a, r32[i]);
        lds_st<us>(sm, L_R32 + 8 + a - (ps[i] & 1) * 2, (us)r16[i]);
      }
    }
    lds_signal(sm, M_EDONE, (cbad || __any(!(em <= 8192.f))) ? 0x101u : 1u);
  }

  if (wave < 4) {
    lds_wait_ge(sm, M_EDONE, 7u);
    ST(0, 7)
    const unsigned dn = lds_poll(sm, M_EDONE);
    const rsrc_t rch = mk_rsrc(ws + kOffRec + (size_t)b * kChunkB, (__builtin_amdgcn_readlane(inc, 63) * 12 + 15) & ~15);
    u4 v[3];
#pragma unroll
    for (int i = 0; i < 3; ++i) v[i] = lds_ld<u4>(sm, L_R32 + i * 4096 + t * 16);
#pragma unroll
    for (int i = 0; i < 3; ++i) __builtin_amdgcn_raw_buffer_store_b128(v[i], rch, i * 4096 + t * 16, 0, kSC1);
    lds_signal(sm, M_WDONE, 1u);
    const unsigned k0 = g.x ^ 0x9E3779B9u, k1 = g.y ^ 0x7F4A7C15u;
    const unsigned lo = (k0 << 23) | ((dn >> 8) ? 1u << 22 : 0u), hi = (k0 >> 9) | (k1 << 23);
    u4 gr = {lo | (c0 << 11) | ex0, hi, lo | (c1 << 11) | ex1, hi};
    asm volatile("" : "+v"(gr));
    vm_drain();
    if (lds_ticket(sm, M_PDONE) == 3u)
      __builtin_amdgcn_raw_buffer_store_b128(gr, mk_rsrc(ws + kOffSeg + b * kSegB, 1024), lane * 16, 0, kSC1);
    ST(0, 2)
  }
  if (wave >= 4) {
    __builtin_amdgcn_s_setprio(3);
    if (wave < 8 || wave == 8 || wave == 10) {
      const unsigned tasks = wave == 4 ? 0xf41u : wave == 5 ? 0xf52u : wave == 6 ? 0xf6u : wave == 7 ? 0xf30u : wave == 8 ? 0xf416u : 0xf5320u;
      const int h = wave >> 3;
      proj<true>(sm, ws, lane, tasks, h, gs, n0, nb ST_ARG);
      lds_wait_ge(sm, M_EDONE, 7u);
      u2 f = table_tag(sm);
      asm volatile("" : "+v"(f));
      vm_drain();
      ST(0, 6)
      const char* flags = ws + kOffFlag + (gs * 2 + h) * 16384;
      if (lds_ticket(sm, M_TDONE + h) == (h ? 1u : 3u) && lane == 0) st_agent((char*)flags + bucket * 128, f);
      if (wave >= 6) {
        const int j = h ? (wave >> 1) & 1 : wave & 1;
        lds_wait_ge(sm, M_TDONE + h, h ? 2u : 4u);
        if (wave == 6) STL(51)
        if (wave == 8) STL(52)
        __builtin_amdgcn_s_sleep(FDELAY);
        if (h) __builtin_amdgcn_s_sleep(FDELAY);
        wait_words(flags + (j * 64 + lane) * 128, f.x, ~0u, f.y, abort);
        lds_signal(sm, M_GO + h, abort ? 1u + kAbort : 1u);
        ST(0, 7)
        if (h == 0) {
          const rsrc_t rt1 = mk_rsrc(ws + kOffTab + (size_t)(gs * 2) * kRows * 128, kN * 128);
#pragma unroll 1
          for (int r = 0; r < PF1; ++r) touch_lines(rt1, (j * 12544 + r * 2048 + (b >> 3) * 64 + lane) * 128, la + L_JUNK);
        }
      }
    } else {
      const int h = wave >= 11 && wave < 14;
      const unsigned tasks = wave < 12 ? 0xf526u : (wave & 1) ? 0xf41u : 0xf30u;
      slow = proj<false>(sm, ws, lane, tasks, h, gs, n0, nb ST_ARG);
      lds_signal(sm, M_GO + h, __any(slow) ? 1u + kSlow : 1u);
    }
  }
  if (wave < 4) {
    const u2 g = lds_ld<u2>(sm, L_MISC + M_G0 * 4);
    const unsigned k0 = g.x ^ 0x9E3779B9u, k1 = g.y ^ 0x7F4A7C15u;
    __builtin_amdgcn_s_sleep(GDELAY);
    const unsigned gx = wait_words(ws + kOffSeg + t * kSegB + bucket * 8, k0 << 23, 0xff800000u, (k0 >> 9) | (k1 << 23), abort);
    ST(1, 0)
    slow |= (gx >> 22) & 1u;
    const rsrc_t rrec = mk_rsrc(ws + kOffRec, 256 * kChunkB);
    const unsigned cn = (gx >> 11) & 0x7ffu, bo = t * kChunkB + (gx & 0x7ffu) * 12;
    const unsigned l12 = (lane & 7) * 12, l2 = (lane & 7) * 2;
    u3 rc[9];
    int rem[9];
#define REC_LOAD(I)                                                                          \
    {                                                                                        \
      rem[I] = oct_bcast<I>(cn) - l2;                                                        \
      const unsigned o = oct_bcast<I>(bo) + l12;                                             \
      rc[I] = __builtin_amdgcn_raw_buffer_load_b96(rrec, rem[I] > 0 ? o : kOOB, 0, kSC1);    \
    }
    REC_LOAD(0) REC_LOAD(1) REC_LOAD(2) REC_LOAD(3) REC_LOAD(4) REC_LOAD(5) REC_LOAD(6) REC_LOAD(7)
#undef REC_LOAD
    if (wave == 1) STL(44)
    {
      const int oc = max((int)cn - 16, 0), ov = (oc + 1) >> 1, oi = wave_scan(ov);
      const unsigned lst = L_ORD + wave * 384, tot = __builtin_amdgcn_readlane(oi, 63);
      unsigned p = min(oi - ov, 64);
      int k = 0;
      do {
#pragma unroll
        for (int m = 0; m < 4; ++m, ++k)
          lds_st<unsigned>(sm, lst + (k < ov ? min(p + k, 64u) : 64u + (lane & 31)) * 4, (bo + 96u + 12u * k) | (min(oc - 2 * k, 2) << 24));
      } while (__any(k < ov) && k < 64);
      lds_order();
      const unsigned en = lds_ld<unsigned>(sm, lst + lane * 4);
      slow |= tot > 64u;
      rem[8] = (unsigned)lane < tot ? en >> 24 : 0;
      rc[8] = __builtin_amdgcn_raw_buffer_load_b96(rrec, (unsigned)lane < tot ? en & 0xffffffu : kOOB, 0, kSC1);
    }
    if (wave == 1) STL(45)
    lds_wait_ge(sm, M_WDONE, 16u);
    unsigned ad[18];
    const unsigned spare = 800u + (lane & 31) * 4;
#pragma unroll
    for (int i = 0; i < 9; ++i) {
      ad[2 * i] = lds_add(sm, L_CNT + (rem[i] > 0 ? rc[i][2] & 0xffffu : spare), 4u);
      ad[2 * i + 1] = lds_add(sm, L_CNT + (rem[i] > 1 ? rc[i][2] >> 16 : spare), 4u);
    }
    lds_signal(sm, M_RDONE, 1u);
    ST(1, 1)
#pragma unroll
    for (int i = 0; i < 18; ++i) lds_st<unsigned>(sm, min(ad[i], (unsigned)L_TRASH), rc[i >> 1][i & 1]);
    lds_wait_ge(sm, M_RDONE, 4u);
    ST(1, 2)
    const int j = wave * 50 + min(lane, 49);
    const bool nv = lane < 50 && j < ng * 8;
    unsigned dg = (lds_ld<unsigned>(sm, L_CNT + j * 4) - (L_ROWS + 256 + j * kRowB)) >> 2, rk = 0;
    slow |= dg > (unsigned)kCap;
    dg = min(dg, 63u);
    if (nv) rk = lds_add(sm, L_HIST + dg * 4, 1u);
    lds_signal(sm, M_SORT, 1u);
    if (b == 0 && t == 0) {
      const u64 gn = (((u64)g.y << 32) | g.x) + 1;
      st_agent(ws + kOffEpoch, u2{(unsigned)gn, (unsigned)(gn >> 32)});
    }
    lds_wait_ge(sm, M_SORT, 4u);
    const unsigned above = ng * 8 - wave_scan(lds_ld<unsigned>(sm, L_HIST + lane * 4));
    const unsigned pos = __builtin_amdgcn_ds_bpermute(dg << 2, above) + rk;
    if (nv) lds_st<u2>(sm, L_ORD + pos * 8, u2{j | (dg << 16), la + L_X + j * 256 + ((j & 15) << 4)});
    lds_signal(sm, M_GO, 1u + (__any(slow) ? kSlow : 0u) + (abort ? kAbort : 0u));
    ST(1, 6)
  }
  const int q = lane & 7;
  unsigned qoff = q * 16, ordb = la + L_ORD + (lane >> 3) * 8, baseq = la + L_ROWS + q * 16;
  unsigned xsk = ((gs * 8 + (q >> 1)) << 4) | ((q & 1) << 3), xoff = gs * 128 + q * 8;
  asm volatile("" : "+v"(qoff), "+v"(ordb), "+v"(baseq), "+v"(xsk), "+v"(xoff));
  ST(1, 3)
  unsigned go;
  while (((go = lds_poll(sm, M_GO)) & 0xffu) < 9u) __builtin_amdgcn_s_sleep(1);
  lds_order();
  ST(1, 4)
  int pass = 0;
  if (go < kSlow) {
    const rsrc_t ro = mk_rsrc(out + (size_t)n0 * 128, nb * 256);
    f4 cn = lds_ld<f4>(sm, L_CF + qoff);
    STC(0)
    int gi = wave < 8 ? wave : wave == 9 ? 8 : wave >= 14 ? wave - 5 : lds_ticket(sm, M_QUEUE);
    while (gi < 2 * ng) {
      if (!pass && gi >= ng) {
        while (((go = lds_poll(sm, M_GO2)) & 0xffu) < 5u) __builtin_amdgcn_s_sleep(1);
        lds_order();
        if (go >= kSlow) break;
        pass = 1;
        cn = lds_ld<f4>(sm, L_CF + 128 + qoff);
        ordb -= ng * 64;
        baseq += 128;
        xsk += 64;
        xoff += 64;
        if (gi == ng) STL(56)
      }
      const rsrc_t rtab = mk_rsrc(ws + kOffTab + (size_t)(gs * 2 + pass) * kRows * 128, kN * 128);
      int bad;
      edge_groups(gi, bad, pass ? 2 * ng : ng, RTAB, ro, cn, qoff, ordb, la + L_ROWS, baseq, xsk, xoff, la + L_MISC + M_QUEUE * 4);
      if (bad < 0) continue;
      const u2 ent = lds_ld<u2>(sm, ordb - la + bad * 64);
      const unsigned node = ent.x & 0xffffu, row = L_ROWS + node * kRowB;
      const u4 au = lds_ld<u4>(sm, baseq - la + node * kRowB);
      const u2 xs = lds_ld<u2>(sm, (ent.y ^ xsk) - la);
      f2 n01 = {0.f, 0.f}, n23 = {0.f, 0.f}, s01 = {1e-6f, 1e-6f}, s23 = {1e-6f, 1e-6f};
#pragma unroll 1
      for (unsigned k = 0; k < (ent.x >> 16); ++k) {
        const unsigned rc = lds_ld<unsigned>(sm, row + 256 + k * 4);
        const u4 v = gather(rtab, rc, qoff);
        f2 d01, d23;
        gate_den(v, rc, cn, f2{u2f(au.x), u2f(au.y)}, f2{u2f(au.z), u2f(au.w)}, d01, d23);
        const f2 g01 = {__builtin_amdgcn_rcpf(d01.x), __builtin_amdgcn_rcpf(d01.y)};
        const f2 g23 = {__builtin_amdgcn_rcpf(d23.x), __builtin_amdgcn_rcpf(d23.y)};
        n01 = pk_fma(g01, f2{u2f(v.x), u2f(v.y)}, n01);
        n23 = pk_fma(g23, f2{u2f(v.z), u2f(v.w)}, n23);
        s01 += g01;
        s23 += g23;
      }
      const f2 z01 = pk_fma(n01, f2{__builtin_amdgcn_rcpf(s01.x), __builtin_amdgcn_rcpf(s01.y)}, f2{bf_lo(au.x), bf_lo(au.y)});
      const f2 z23 = pk_fma(n23, f2{__builtin_amdgcn_rcpf(s23.x), __builtin_amdgcn_rcpf(s23.y)}, f2{bf_lo(au.z), bf_lo(au.w)});
      const f2 o01 = f2{relu(z01.x), relu(z01.y)} + f2{bf_lo(xs.x), bf_hi(xs.x)};
      const f2 o23 = f2{relu(z23.x), relu(z23.y)} + f2{bf_lo(xs.y), bf_hi(xs.y)};
      __builtin_amdgcn_raw_buffer_store_b64(u2{pack_bf(o01.x, o01.y), pack_bf(o23.x, o23.y)}, ro, node * 256 + xoff, 0, 0);
      vm_drain();
    }
    ST(1, 7)
    STC(1)
    ST_FLUSH
    if (pass) return;
    while (((go = lds_poll(sm, M_GO2)) & 0xffu) < 5u) __builtin_amdgcn_s_sleep(1);
    if (go < kSlow) return;
    pass = 2;
  }

  wg_barrier();
  if ((lds_poll(sm, M_GO) | lds_poll(sm, M_GO2)) >= kAbort) return;
#pragma unroll 1
  for (; pass < 4; ++pass) {
    const int fb = gs * 64 + pass * 16;
    const rsrc_t rtab = mk_rsrc(ws + kOffTab + (size_t)(fb >> 5) * kRows * 128, kN * 128);
#pragma unroll 1
    for (int i = t; i < nb * 16; i += 1024) {
      const unsigned ra = L_ROWS + (i >> 4) * kRowB + (i & 15) * 4;
      lds_st<float>(sm, ra, kNL2E * dot128(x + (size_t)(n0 + (i >> 4)) * 128, wa + fb + (i & 15)));
      lds_st<float>(sm, ra + 64, 0.f);
      lds_st<float>(sm, ra + 128, 0.f);
    }
    wg_barrier();
#pragma unroll 1
    for (int e = t; e < kE; e += 1024) {
      const unsigned dl = (unsigned)(dst[e] - n0);
      if (dl < (unsigned)nb) {
        const int sn = src[e];
        const float ev = bf_lo(ea[e]);
#pragma unroll 1
        for (int k = 0; k < 16; ++k) {
          const unsigned w = __builtin_amdgcn_raw_buffer_load_b32(rtab, sn * 128 + ((fb & 16) + k) * 4, 0, kSC1);
          float bv = h_lo(w);
          if (!(__builtin_fabsf(bv) <= 65504.f)) bv = kNL2E * dot128(x + (size_t)sn * 128, wb + fb + k);
          const unsigned ra = L_ROWS + dl * kRowB + k * 4;
          const float tt = lds_ld<float>(sm, ra) + bv + ev * (kNL2E * bf_lo(wc[fb + k]));
          const float gt = __builtin_amdgcn_rcpf(1.f + __builtin_amdgcn_exp2f(tt));
          __hip_atomic_fetch_add((LAS float*)(sm + ra + 64), gt * u2f(w), __ATOMIC_RELAXED, __HIP_MEMORY_SCOPE_WORKGROUP);
          __hip_atomic_fetch_add((LAS float*)(sm + ra + 128), gt, __ATOMIC_RELAXED, __HIP_MEMORY_SCOPE_WORKGROUP);
        }
      }
    }
    wg_barrier();
#pragma unroll 1
    for (int i = t; i < nb * 16; i += 1024) {
      const unsigned ra = L_ROWS + (i >> 4) * kRowB + (i & 15) * 4;
      const size_t xr = (size_t)(n0 + (i >> 4)) * 128;
      const float z = dot128(x + xr, wu + fb + (i & 15)) + lds_ld<float>(sm, ra + 64) / (lds_ld<float>(sm, ra + 128) + 1e-6f);
      out[xr + fb + (i & 15)] = (us)(pack_bf(bf_lo(x[xr + fb + (i & 15)]) + relu(z), 0.f) & 0xffffu);
    }
    wg_barrier();
  }
}

}

extern "C" void kernel_launch(void* const* d_in, const int* in_sizes, int n_in, void* d_out, int out_size,
                              void* d_ws, size_t ws_size, hipStream_t stream) {
  k_gcn<<<256, 1024, 0, stream>>>((const us*)d_in[0], (const us*)d_in[1], (const int*)d_in[2], (const int*)d_in[3],
                                  (const us*)d_in[4], (const us*)d_in[5], (const us*)d_in[6], (const us*)d_in[7],
                                  (const us*)d_in[8], (char*)d_ws, (us*)d_out);
}
```
